```python
import torch

def _softmax(x, axis=-1):
    return torch.softmax(x, dim=axis)
def _gelu(x):
    return torch.nn.functional.gelu(x, approximate="tanh")
def _silu(x):
    return torch.nn.functional.silu(x)
def _sigmoid(x):
    return torch.sigmoid(x)
def _layernorm(x, axis=-1, eps=1e-5):
    m = x.mean(dim=axis, keepdim=True)
    v = x.var(dim=axis, keepdim=True, unbiased=False)
    return (x - m) / torch.sqrt(v + eps)
def _scatter_add(tgt, idx, src, axis=0):
    return tgt.clone().index_add_(axis, idx, src)
def _segment_sum(src, seg, n):
    out = torch.zeros((n,) + src.shape[1:], dtype=src.dtype)
    return out.index_add_(0, seg, src)
def _pad(x, pw):
    flat = tuple(v for pair in reversed(pw) for v in pair)
    return torch.nn.functional.pad(x, flat)

kernel_name = 'sequential_preLN__spectral_fnet__moe_arch_mod__prompt'

def setup_inputs():

    _w = (lambda a: (lambda t: t.to(torch.bfloat16) if t.is_floating_point() else t)(torch.as_tensor(a)))
    def _A_setup():
        B, S, D = 1, 2048, 448
        ns = torch.arange(S)[:, None] * torch.arange(S)[None, :]
        nd = torch.arange(D)[:, None] * torch.arange(D)[None, :]
        Ws_re = torch.cos(-2.0 * torch.pi * ns / S).to(float)
        Ws_im = torch.sin(-2.0 * torch.pi * ns / S).to(float)
        Wd_re = torch.cos(-2.0 * torch.pi * nd / D).to(float)
        Wd_im = torch.sin(-2.0 * torch.pi * nd / D).to(float)
        return {
            "x": _w((lambda *s: torch.randn(*s, dtype=torch.bfloat16) * 0.05)(B, S, D)),
            "dft_seq_re": _w(Ws_re),
            "dft_seq_im": _w(Ws_im),
            "dft_hid_re": _w(Wd_re),
            "dft_hid_im": _w(Wd_im),
        }
    _A_in = _A_setup()

    def _B_setup():
        B, S, D, F, C = 1, 2048, 448, 1792, 8
        return {
            "x": _w((lambda *s: torch.randn(*s, dtype=torch.bfloat16) * 0.05)(B, S, D)),
            "w_r": _w((lambda *s: torch.randn(*s, dtype=torch.bfloat16) * 1.6)(D)),
            "w_gate": _w((lambda *s: torch.randn(*s, dtype=torch.bfloat16) * 0.4)(D, F)),
            "w_up": _w((lambda *s: torch.randn(*s, dtype=torch.bfloat16) * 0.4)(D, F)),
            "w_down": _w((lambda *s: torch.randn(*s, dtype=torch.bfloat16) * 0.4)(F, D)),
        }
    _B_in = _B_setup()

    return {
        "A_x": _A_in["x"],
        "A_dft_seq_re": _A_in["dft_seq_re"],
        "A_dft_seq_im": _A_in["dft_seq_im"],
        "A_dft_hid_re": _A_in["dft_hid_re"],
        "A_dft_hid_im": _A_in["dft_hid_im"],
        "B_x": _B_in["x"],
        "B_w_r": _B_in["w_r"],
        "B_w_gate": _B_in["w_gate"],
        "B_w_up": _B_in["w_up"],
        "B_w_down": _B_in["w_down"],
    }

def reference(A_x, A_dft_seq_re, A_dft_seq_im, A_dft_hid_re, A_dft_hid_im, B_x, B_w_r, B_w_gate, B_w_up, B_w_down):
    def _A_ref(x, dft_seq_re, dft_seq_im, dft_hid_re, dft_hid_im):
        a = torch.einsum("bsd,ed->bse", x, dft_hid_re)
        b = torch.einsum("bsd,ed->bse", x, dft_hid_im)
        y = torch.einsum("ts,bse->bte", dft_seq_re, a) - torch.einsum("ts,bse->bte", dft_seq_im, b)
        return y

    def _B_ref(x, w_r, w_gate, w_up, w_down):
        B, S, D = x.shape
        C = 8
        r = x @ w_r
        idx = torch.argsort(-r, axis=-1)[:, :C]
        idx_e = (lambda x, n, axis: torch.repeat_interleave(x, n, dim=axis))(idx.reshape(B, C, 1), D, axis=2)
        x_sel = (lambda x, i, axis: torch.take_along_dim(x, i, dim=axis))(x, idx_e, axis=1)
        g = _silu(x_sel @ w_gate)
        u = x_sel @ w_up
        y = (g * u) @ w_down
        r_sel = _sigmoid((lambda x, i, axis: torch.take_along_dim(x, i, dim=axis))(r, idx, axis=1))
        y = y * r_sel.reshape(B, C, 1)
        b_off = (torch.arange(B) * S).reshape(B, 1)
        flat_idx = (idx + b_off).reshape(B * C)
        zero = (x * 0.0).reshape(B * S, D)
        upd = _scatter_add(zero, flat_idx, y.reshape(B * C, D), axis=0)
        return x + upd.reshape(B, S, D)

    x = A_x
    h = x + _A_ref(_layernorm(x, axis=-1), A_dft_seq_re, A_dft_seq_im, A_dft_hid_re, A_dft_hid_im)
    h2 = h + _B_ref(_layernorm(h, axis=-1), B_w_r, B_w_gate, B_w_up, B_w_down)
    return h2
```

```cpp
#include <hip/hip_runtime.h>
#include <hip/hip_bf16.h>
#include <stdint.h>

typedef short bf16x8 __attribute__((ext_vector_type(8)));
typedef float f32x4 __attribute__((ext_vector_type(4)));
typedef unsigned v4u __attribute__((ext_vector_type(4)));
typedef unsigned v2u __attribute__((ext_vector_type(2)));
typedef unsigned short u16;

constexpr int S = 2048, D = 448, F = 1792, C = 8;

__device__ __forceinline__ float bf2f(unsigned b) { return __builtin_bit_cast(float, b << 16); }
__device__ __forceinline__ unsigned f2bf(float f) {
  __hip_bfloat16 h = __float2bfloat16(f);
  return (unsigned)__builtin_bit_cast(u16, h);
}
__device__ __forceinline__ float rbf(float f) { return bf2f(f2bf(f)); }
__device__ __forceinline__ unsigned pk(float lo, float hi) { return f2bf(lo) | (f2bf(hi) << 16); }
__device__ __forceinline__ f32x4 mfma(v4u a, v4u b, f32x4 c) {
  return __builtin_amdgcn_mfma_f32_16x16x32_bf16(__builtin_bit_cast(bf16x8, a), __builtin_bit_cast(bf16x8, b), c, 0, 0, 0);
}
__device__ __forceinline__ float lo16(unsigned u) { return __builtin_bit_cast(float, u << 16); }
__device__ __forceinline__ float hi16(unsigned u) { return __builtin_bit_cast(float, u & 0xFFFF0000u); }

constexpr int LROW = 912;
__global__ __launch_bounds__(448) void k1(const u16* __restrict__ x, const u16* __restrict__ wre, const u16* __restrict__ wim,
                                          u16* __restrict__ Bf) {
  __shared__ __attribute__((aligned(16))) unsigned char lds[64 * LROW];
  const int tid = threadIdx.x, lane = tid & 63, w = tid >> 6;
  const int st = blockIdx.x >> 3, ns = blockIdx.x & 7;
  const int s0 = st * 64;
  for (int r = w; r < 64; r += 7) {
    float v[8];
    v4u raw = {0, 0, 0, 0};
    if (lane < 56) raw = *(const v4u*)(x + (size_t)(s0 + r) * D + lane * 8);
    v[0] = lo16(raw.x); v[1] = hi16(raw.x); v[2] = lo16(raw.y); v[3] = hi16(raw.y);
    v[4] = lo16(raw.z); v[5] = hi16(raw.z); v[6] = lo16(raw.w); v[7] = hi16(raw.w);
    float sum = 0.f;
    for (int j = 0; j < 8; ++j) sum += v[j];
    for (int o = 32; o > 0; o >>= 1) sum += __shfl_xor(sum, o);
    const float mean = sum * (1.f / D);
    float sq = 0.f;
    if (lane < 56) for (int j = 0; j < 8; ++j) { float d = v[j] - mean; sq += d * d; }
    for (int o = 32; o > 0; o >>= 1) sq += __shfl_xor(sq, o);
    const float mb = rbf(mean);
    const float sd = rbf(sqrtf(rbf(rbf(sq * (1.f / D)) + 1e-5f)));
    v4u o;
    o.x = pk(rbf(v[0] - mb) / sd, rbf(v[1] - mb) / sd);
    o.y = pk(rbf(v[2] - mb) / sd, rbf(v[3] - mb) / sd);
    o.z = pk(rbf(v[4] - mb) / sd, rbf(v[5] - mb) / sd);
    o.w = pk(rbf(v[6] - mb) / sd, rbf(v[7] - mb) / sd);
    if (lane < 56) *(v4u*)(lds + r * LROW + lane * 16) = o;
  }
  __syncthreads();
  const int part = ns >> 2;
  const int nt = (ns & 3) * 7 + w;
  const u16* W = part ? wim : wre;
  const int col = lane & 15, q = lane >> 4;
  f32x4 acc[4];
  for (int i = 0; i < 4; ++i) acc[i] = f32x4{0, 0, 0, 0};
  const int prow = 8 * (col >> 2) + (col & 3);
#pragma unroll
  for (int kk = 0; kk < 14; ++kk) {
    v4u b = *(const v4u*)(W + (size_t)(nt * 16 + col) * D + kk * 32 + q * 8);
#pragma unroll
    for (int t = 0; t < 4; ++t) {
      const int row = (t >> 1) * 32 + prow + (t & 1) * 4;
      v4u a = *(const v4u*)(lds + row * LROW + kk * 64 + q * 16);
      acc[t] = mfma(a, b, acc[t]);
    }
  }
#pragma unroll
  for (int g = 0; g < 2; ++g) {
    v4u o;
    o.x = pk(acc[2 * g][0], acc[2 * g][1]);
    o.y = pk(acc[2 * g][2], acc[2 * g][3]);
    o.z = pk(acc[2 * g + 1][0], acc[2 * g + 1][1]);
    o.w = pk(acc[2 * g + 1][2], acc[2 * g + 1][3]);
    const int ks = st * 2 + g;
    *(v4u*)(Bf + ((size_t)((part * 64 + ks) * 28 + nt) * 64 + lane) * 8) = o;
  }
}

__global__ __launch_bounds__(256, 1) void k2(const u16* __restrict__ wsre, const u16* __restrict__ wsim, const u16* __restrict__ Bf,
                                             u16* __restrict__ Y) {
  __shared__ __attribute__((aligned(16))) float red[3 * 28 * 256];
  const int tid = threadIdx.x, lane = tid & 63, w = tid >> 6;
  const int b = blockIdx.x;
  const int xcd = b & 7, loc = b >> 3;
  const int part = xcd & 1;
  const int rt = (xcd >> 1) * 8 + (loc >> 2);
  const int cs = loc & 3;
  const u16* A = (part ? wsim : wsre) + (size_t)(rt * 64) * S;
  const int row = lane & 15, q = lane >> 4;
  f32x4 acc[4][7];
#pragma unroll
  for (int i = 0; i < 4; ++i)
#pragma unroll
    for (int j = 0; j < 7; ++j) acc[i][j] = f32x4{0, 0, 0, 0};
  const u16* ap = A + (size_t)row * S + w * 512 + q * 8;
  const u16* bp = Bf + ((size_t)((part * 64 + w * 16) * 28 + cs * 7) * 64 + lane) * 8;
#pragma unroll 2
  for (int ks = 0; ks < 16; ++ks) {
    v4u a[4], bb[7];
#pragma unroll
    for (int i = 0; i < 4; ++i) a[i] = *(const v4u*)(ap + (size_t)i * 16 * S + ks * 32);
#pragma unroll
    for (int j = 0; j < 7; ++j) bb[j] = *(const v4u*)(bp + (size_t)(ks * 28 + j) * 512);
#pragma unroll
    for (int i = 0; i < 4; ++i)
#pragma unroll
      for (int j = 0; j < 7; ++j) acc[i][j] = mfma(a[i], bb[j], acc[i][j]);
  }
  if (w > 0) {
#pragma unroll
    for (int i = 0; i < 4; ++i)
#pragma unroll
      for (int j = 0; j < 7; ++j) *(f32x4*)(red + ((w - 1) * 28 + i * 7 + j) * 256 + lane * 4) = acc[i][j];
  }
  __syncthreads();
  if (w == 0) {
  }
  if (w == 0) {
    u16* Yp = Y + (size_t)part * S * D;
#pragma unroll
    for (int i = 0; i < 4; ++i)
#pragma unroll
      for (int j = 0; j < 7; ++j) {
        f32x4 v = acc[i][j];
#pragma unroll
        for (int ww = 0; ww < 3; ++ww) v += *(const f32x4*)(red + (ww * 28 + i * 7 + j) * 256 + lane * 4);
#pragma unroll
        for (int r = 0; r < 4; ++r)
          Yp[(size_t)(rt * 64 + i * 16 + q * 4 + r) * D + cs * 112 + j * 16 + row] = (u16)f2bf(v[r]);
      }
  }
}

__global__ __launch_bounds__(256) void k3(const u16* __restrict__ x, const u16* __restrict__ Y, const u16* __restrict__ wr,
                                          u16* __restrict__ out, u16* __restrict__ hbuf, u16* __restrict__ lnbuf, float* __restrict__ rbuf) {
  const int lane = threadIdx.x & 63, w = threadIdx.x >> 6;
  for (int rr = 0; rr < 2; ++rr) {
    const int t = blockIdx.x * 8 + w * 2 + rr;
    v4u xr = {0, 0, 0, 0}, yr = {0, 0, 0, 0}, yi = {0, 0, 0, 0}, wv = {0, 0, 0, 0};
    if (lane < 56) {
      xr = *(const v4u*)(x + (size_t)t * D + lane * 8);
      yr = *(const v4u*)(Y + (size_t)t * D + lane * 8);
      yi = *(const v4u*)(Y + (size_t)(S + t) * D + lane * 8);
      wv = *(const v4u*)(wr + lane * 8);
    }
    float h[8];
    unsigned xa[4] = {xr.x, xr.y, xr.z, xr.w}, ra[4] = {yr.x, yr.y, yr.z, yr.w}, ia[4] = {yi.x, yi.y, yi.z, yi.w}, wa[4] = {wv.x, wv.y, wv.z, wv.w};
    for (int j = 0; j < 4; ++j) {
      h[2 * j] = rbf(lo16(xa[j]) + rbf(lo16(ra[j]) - lo16(ia[j])));
      h[2 * j + 1] = rbf(hi16(xa[j]) + rbf(hi16(ra[j]) - hi16(ia[j])));
    }
    float sum = 0.f;
    for (int j = 0; j < 8; ++j) sum += h[j];
    for (int o = 32; o > 0; o >>= 1) sum += __shfl_xor(sum, o);
    const float mean = sum * (1.f / D);
    float sq = 0.f;
    if (lane < 56) for (int j = 0; j < 8; ++j) { float d = h[j] - mean; sq += d * d; }
    for (int o = 32; o > 0; o >>= 1) sq += __shfl_xor(sq, o);
    const float mb = rbf(mean);
    const float sd = rbf(sqrtf(rbf(rbf(sq * (1.f / D)) + 1e-5f)));
    float l[8];
    float dot = 0.f;
    for (int j = 0; j < 8; ++j) l[j] = rbf(rbf(h[j] - mb) / sd);
    for (int j = 0; j < 4; ++j) dot += l[2 * j] * lo16(wa[j]) + l[2 * j + 1] * hi16(wa[j]);
    if (lane >= 56) dot = 0.f;
    for (int o = 32; o > 0; o >>= 1) dot += __shfl_xor(dot, o);
    if (lane < 56) {
      v4u o, hh, ll;
      o.x = pk(h[0] + l[0], h[1] + l[1]); o.y = pk(h[2] + l[2], h[3] + l[3]);
      o.z = pk(h[4] + l[4], h[5] + l[5]); o.w = pk(h[6] + l[6], h[7] + l[7]);
      hh.x = pk(h[0], h[1]); hh.y = pk(h[2], h[3]); hh.z = pk(h[4], h[5]); hh.w = pk(h[6], h[7]);
      ll.x = pk(l[0], l[1]); ll.y = pk(l[2], l[3]); ll.z = pk(l[4], l[5]); ll.w = pk(l[6], l[7]);
      *(v4u*)(out + (size_t)t * D + lane * 8) = o;
      *(v4u*)(hbuf + (size_t)t * D + lane * 8) = hh;
      *(v4u*)(lnbuf + (size_t)t * D + lane * 8) = ll;
    }
    if (lane == 0) rbuf[t] = rbf(dot);
  }
}

__device__ __forceinline__ unsigned sort_key(float r, int idx) {
  unsigned u = __builtin_bit_cast(unsigned, r) >> 16;
  u = (u & 0x8000u) ? (~u & 0xFFFFu) : (u | 0x8000u);
  return (u << 16) | (unsigned)(0xFFFF - idx);
}
__device__ void top8(const float* __restrict__ rbuf, int* sidx, unsigned* sc) {
  const int tid = threadIdx.x, lane = tid & 63, w = tid >> 6;
  unsigned k[8];
  for (int j = 0; j < 8; ++j) k[j] = sort_key(rbuf[tid * 8 + j], tid * 8 + j);
  for (int it = 0; it < 8; ++it) {
    unsigned m = 0;
    for (int j = 0; j < 8; ++j) m = max(m, k[j]);
    unsigned wm = m;
    for (int o = 32; o > 0; o >>= 1) wm = max(wm, (unsigned)__shfl_xor((int)wm, o));
    for (int j = 0; j < 8; ++j) if (k[j] == wm) k[j] = 0;
    if (lane == 0) sc[w * 8 + it] = wm;
  }
  __syncthreads();
  if (tid < 32) {
    unsigned me = sc[tid];
    int rank = 0;
    for (int j = 0; j < 32; ++j) rank += (sc[j] > me) ? 1 : 0;
    if (rank < 8) sidx[rank] = 0xFFFF - (int)(me & 0xFFFFu);
  }
  __syncthreads();
}

__global__ __launch_bounds__(256) void k4(const float* __restrict__ rbuf, const u16* __restrict__ lnbuf, const u16* __restrict__ wg,
                                          const u16* __restrict__ wu, u16* __restrict__ act, int* __restrict__ idxout) {
  __shared__ int sidx[8];
  __shared__ unsigned sc[32];
  __shared__ float xs[8][D];
  __shared__ float pg[16][8][16], pu[16][8][16];
  const int tid = threadIdx.x;
  top8(rbuf, sidx, sc);
  if (blockIdx.x == 0 && tid < 8) idxout[tid] = sidx[tid];
  for (int i = tid; i < 8 * D; i += 256) xs[i / D][i % D] = bf2f(lnbuf[(size_t)sidx[i / D] * D + i % D]);
  __syncthreads();
  const int f = blockIdx.x * 16 + (tid & 15), dg = tid >> 4;
  float g[8], u[8];
  for (int c = 0; c < 8; ++c) g[c] = u[c] = 0.f;
  for (int d = dg * 28; d < dg * 28 + 28; ++d) {
    const float a = bf2f(wg[(size_t)d * F + f]), bb = bf2f(wu[(size_t)d * F + f]);
    for (int c = 0; c < 8; ++c) { g[c] += xs[c][d] * a; u[c] += xs[c][d] * bb; }
  }
  for (int c = 0; c < 8; ++c) { pg[dg][c][tid & 15] = g[c]; pu[dg][c][tid & 15] = u[c]; }
  __syncthreads();
  if (tid < 128) {
    const int c = tid >> 4, ff = tid & 15;
    float gs = 0.f, us = 0.f;
    for (int j = 0; j < 16; ++j) { gs += pg[j][c][ff]; us += pu[j][c][ff]; }
    gs = rbf(gs); us = rbf(us);
    const float si = rbf(gs / (1.f + __expf(-gs)));
    act[c * F + blockIdx.x * 16 + ff] = (u16)f2bf(si * us);
  }
}

__global__ __launch_bounds__(256) void k5(const u16* __restrict__ act, const u16* __restrict__ wd, const int* __restrict__ idx,
                                          const float* __restrict__ rbuf, const u16* __restrict__ hbuf, const u16* __restrict__ lnbuf,
                                          u16* __restrict__ out) {
  __shared__ float pp[16][8][16];
  const int tid = threadIdx.x;
  const int e = blockIdx.x * 16 + (tid & 15), fg = tid >> 4;
  float y[8];
  for (int c = 0; c < 8; ++c) y[c] = 0.f;
  for (int f = fg * 112; f < fg * 112 + 112; ++f) {
    const float a = bf2f(wd[(size_t)f * D + e]);
    for (int c = 0; c < 8; ++c) y[c] += bf2f(act[c * F + f]) * a;
  }
  for (int c = 0; c < 8; ++c) pp[fg][c][tid & 15] = y[c];
  __syncthreads();
  if (tid < 128) {
    const int c = tid >> 4, ee = blockIdx.x * 16 + (tid & 15);
    float s = 0.f;
    for (int j = 0; j < 16; ++j) s += pp[j][c][tid & 15];
    const int t = idx[c];
    const float r = rbuf[t];
    const float sg = rbf(1.f / (1.f + __expf(-r)));
    const float yy = rbf(rbf(s) * sg);
    const float ln = bf2f(lnbuf[(size_t)t * D + ee]), h = bf2f(hbuf[(size_t)t * D + ee]);
    out[(size_t)t * D + ee] = (u16)f2bf(h + rbf(ln + yy));
  }
}

extern "C" void kernel_launch(void* const* d_in, const int* in_sizes, int n_in, void* d_out, int out_size, void* d_ws, size_t ws_size,
                              hipStream_t stream) {
  const u16* x = (const u16*)d_in[0];
  const u16* wsre = (const u16*)d_in[1];
  const u16* wsim = (const u16*)d_in[2];
  const u16* wre = (const u16*)d_in[3];
  const u16* wim = (const u16*)d_in[4];
  const u16* wr = (const u16*)d_in[6];
  const u16* wg = (const u16*)d_in[7];
  const u16* wu = (const u16*)d_in[8];
  const u16* wd = (const u16*)d_in[9];
  u16* out = (u16*)d_out;
  unsigned char* ws = (unsigned char*)d_ws;
  u16* Bf = (u16*)(ws);
  u16* Y = (u16*)(ws + (4u << 20));
  u16* hbuf = (u16*)(ws + (8u << 20));
  u16* lnbuf = (u16*)(ws + (10u << 20));
  float* rbuf = (float*)(ws + (12u << 20));
  u16* act = (u16*)(ws + (13u << 20));
  int* idx = (int*)(ws + (14u << 20));
  k1<<<256, 448, 0, stream>>>(x, wre, wim, Bf);
  k2<<<256, 256, 0, stream>>>(wsre, wsim, Bf, Y);
  k3<<<256, 256, 0, stream>>>(x, Y, wr, out, hbuf, lnbuf, rbuf);
  k4<<<112, 256, 0, stream>>>(rbuf, lnbuf, wg, wu, act, idx);
  k5<<<28, 256, 0, stream>>>(act, wd, idx, rbuf, hbuf, lnbuf, out);
}
```

```cpp
#include <hip/hip_runtime.h>
#include <hip/hip_bf16.h>
#include <stdint.h>

typedef unsigned short u16;
constexpr unsigned WS_RK = 65536u, WS_ACT = 98304u;


struct Args {
  const u16 *x, *wsre, *wsim, *wre, *wim;
  unsigned char* ws;
  const u16* wr;
  u16* out;
  const u16 *wg, *wu, *wd;
};

__global__ __launch_bounds__(512, 2) void fnet_moe_kernel(Args args) {
  extern __shared__ __attribute__((aligned(16))) unsigned char lds[];
  const void* ka = (const void*)__builtin_amdgcn_kernarg_segment_ptr();
  unsigned tid = threadIdx.x;
  asm volatile(
      "L_kbeg_%=:\n\t"
      "s_load_dwordx8 s[56:63], %[ka], 0x0\n\t"
      "s_load_dwordx4 s[68:71], %[ka], 0x20\n\t"
      "v_and_b32 v104, 63, v0\n\t"
      "v_lshrrev_b32 v116, 6, v0\n\t"
      "s_mov_b32 s43, %[bid]\n\t"
      "v_readfirstlane_b32 s40, v116\n\t"
      "s_and_b32 s41, s43, 7\n\t"
      "s_lshr_b32 s42, s43, 3\n\t"
      "v_and_b32 v105, 15, v104\n\t"
      "v_lshrrev_b32 v106, 4, v104\n\t"
      "v_lshlrev_b32 v107, 4, v104\n\t"
      "v_mov_b32 v126, 1\n\t"
      "v_mov_b32 v127, 160768\n\t"
      "s_cmp_eq_u32 s40, 7\n\t"
      "s_cbranch_scc0 L_nopf_%=\n\t"
      "s_getpc_b64 s[54:55]\n\t"
      "s_and_b32 s54, s54, 0xffffff00\n\t"
      "s_mov_b32 s28, s54\n\t"
      "s_and_b32 s29, s55, 0xffff\n\t"
      "s_mov_b32 s30, L_kend - L_kbeg_%= - 256\n\t"
      "s_mov_b32 s31, 0x20000\n\t"
      "s_mul_i32 s54, s42, 1536\n\t"
      "v_lshlrev_b32 v116, 6, v104\n\t"
      "s_mov_b64 exec, 0xffffff\n\t"
      "buffer_load_dword v103, v116, s[28:31], s54 offen\n\t"
      "s_mov_b64 exec, -1\n\t"
      "L_nopf_%=:\n\t"
      "s_and_b32 s46, s41, 1\n\t"
      "s_and_b32 s47, s42, 3\n\t"
      "s_lshr_b32 s54, s41, 1\n\t"
      "s_lshl_b32 s54, s54, 3\n\t"
      "s_lshr_b32 s55, s42, 2\n\t"
      "s_add_u32 s48, s54, s55\n\t"
      "s_and_b32 s53, s41, 3\n\t"
      "s_lshr_b32 s49, s53, 1\n\t"
      "s_and_b32 s50, s53, 1\n\t"
      "s_lshr_b32 s54, s41, 2\n\t"
      "s_lshl_b32 s54, s54, 3\n\t"
      "s_add_u32 s51, s54, s55\n\t"
      "s_and_b32 s52, s42, 3\n\t"
      "s_and_b32 s54, s48, 7\n\t"
      "s_lshl_b32 s55, s47, 1\n\t"
      "s_add_u32 s54, s54, s55\n\t"
      "s_sub_u32 s55, s54, 7\n\t"
      "s_cmp_lt_u32 s54, 7\n\t"
      "s_cselect_b32 s19, s54, s55\n\t"
      "s_waitcnt lgkmcnt(0)\n\t"
      "s_load_dwordx2 s[44:45], s[70:71], 0x0\n\t"
      "s_mov_b32 s20, s56\n\t"
      "s_and_b32 s21, s57, 0xffff\n\t"
      "s_mov_b32 s22, 1835008\n\t"
      "s_mov_b32 s23, 0x20000\n\t"
      "s_cmp_eq_u32 s46, 0\n\t"
      "s_cselect_b64 s[54:55], s[62:63], s[68:69]\n\t"
      "s_mov_b32 s24, s54\n\t"
      "s_and_b32 s25, s55, 0xffff\n\t"
      "s_mov_b32 s26, 401408\n\t"
      "s_mov_b32 s27, 0x20000\n\t"
      "s_cmp_eq_u32 s49, 0\n\t"
      "s_cselect_b64 s[54:55], s[58:59], s[60:61]\n\t"
      "s_mov_b32 s36, s54\n\t"
      "s_and_b32 s37, s55, 0xffff\n\t"
      "s_mov_b32 s38, 8388608\n\t"
      "s_mov_b32 s39, 0x20000\n\t"
      "s_mov_b32 s64, s70\n\t"
      "s_and_b32 s65, s71, 0xffff\n\t"
      "s_mov_b32 s66, 83886080\n\t"
      "s_mov_b32 s67, 0x20000\n\t"
      "v_lshrrev_b32 v118, 3, v104\n\t"
      "v_lshrrev_b32 v119, 1, v118\n\t"
      "v_and_b32 v120, 7, v104\n\t"
      "v_xor_b32 v119, v119, v120\n\t"
      "v_lshlrev_b32 v119, 4, v119\n\t"
      "v_xor_b32 v120, 64, v119\n\t"
      "v_lshrrev_b32 v116, 1, v105\n\t"
      "v_xor_b32 v116, v116, v106\n\t"
      "v_lshlrev_b32 v116, 4, v116\n\t"
      "v_lshl_add_u32 v124, v105, 7, v116\n\t"
      "v_xor_b32 v125, 64, v124\n\t"
      "s_and_b32 s56, s40, 3\n\t"
      "v_lshrrev_b32 v116, 2, v118\n\t"
      "v_and_b32 v117, 3, v118\n\t"
      "v_lshl_add_u32 v116, v116, 3, v117\n\t"
      "s_mul_i32 s55, s47, 112\n\t"
      "v_add_u32 v117, s55, v116\n\t"
      "v_mul_u32_u24 v117, 896, v117\n\t"
      "v_add_u32 v98, v117, v119\n\t"
      "v_add_u32 v99, v117, v120\n\t"
      "v_add_u32 v99, 14336, v99\n\t"
      "v_mov_b32 v100, v98\n\t"
      "v_mov_b32 v101, v99\n\t"
      "s_cmp_eq_u32 s56, 2\n\t"
      "s_cbranch_scc0 L_nt6_%=\n\t"
      "v_add_u32 v117, s55, v118\n\t"
      "v_mul_u32_u24 v117, 896, v117\n\t"
      "v_add_u32 v100, v117, v119\n\t"
      "v_add_u32 v101, v117, v120\n\t"
      "v_add_u32 v101, 7168, v101\n\t"
      "L_nt6_%=:\n\t"
      "s_lshr_b32 s54, s56, 1\n\t"
      "s_lshl_b32 s54, s54, 5\n\t"
      "s_and_b32 s57, s56, 1\n\t"
      "s_lshl_b32 s57, s57, 2\n\t"
      "s_add_u32 s54, s54, s57\n\t"
      "s_mul_i32 s59, s54, 896\n\t"
      "s_lshl_b32 s55, s48, 6\n\t"
      "s_add_u32 s54, s54, s55\n\t"
      "v_add_u32 v117, s54, v116\n\t"
      "v_mul_u32_u24 v117, 896, v117\n\t"
      "v_add_u32 v96, v117, v119\n\t"
      "v_add_u32 v97, v117, v120\n\t"
      "v_add_u32 v97, 14336, v97\n\t"
      "s_add_u32 s54, s57, 64\n\t"
      "s_cmp_eq_u32 s56, 2\n\t"
      "s_cselect_b32 s54, 96, s54\n\t"
      "s_mul_i32 s60, s54, 896\n\t"
      "s_mov_b64 s[28:29], s[24:25]\n\t"
      "s_mov_b64 s[30:31], s[26:27]\n\t"
      "s_cmp_eq_u32 s56, 3\n\t"
      "s_cselect_b32 s30, 0, s30\n\t"
      "s_lshl_b32 s57, s56, 11\n\t"
      "s_cmp_eq_u32 s56, 3\n\t"
      "s_cselect_b32 s74, 0, 14336\n\t"
      "s_cmp_lt_u32 s40, 4\n\t"
      "s_cbranch_scc0 L_isshi_%=\n\t"
      "s_add_u32 s55, s57, 8192\n\t"
      "s_cmp_eq_u32 s56, 3\n\t"
      "s_cselect_b32 s55, 157696, s55\n\t"
      "s_add_u32 s15, s19, 0\n\t"
      "s_sub_u32 s14, s15, 7\n\t"
      "s_cmp_lt_u32 s15, 7\n\t"
      "s_cselect_b32 s15, s15, s14\n\t"
      "s_lshl_b32 s15, s15, 7\n\t"
      "s_add_u32 m0, s57, 0\n\t"
      "s_add_u32 s54, s59, s15\n\t"
      "buffer_load_dwordx4 v98, s[24:27], s54 offen lds\n\t"
      "s_add_u32 m0, s57, 1024\n\t"
      "s_nop 0\n\t"
      "buffer_load_dwordx4 v99, s[24:27], s54 offen lds\n\t"
      "s_mov_b32 m0, s55\n\t"
      "s_add_u32 s54, s60, s15\n\t"
      "buffer_load_dwordx4 v100, s[28:31], s54 offen lds\n\t"
      "s_add_u32 m0, s55, 1024\n\t"
      "s_add_u32 s55, s55, s74\n\t"
      "buffer_load_dwordx4 v101, s[28:31], s54 offen lds\n\t"
      "s_add_u32 s15, s19, 1\n\t"
      "s_sub_u32 s14, s15, 7\n\t"
      "s_cmp_lt_u32 s15, 7\n\t"
      "s_cselect_b32 s15, s15, s14\n\t"
      "s_lshl_b32 s15, s15, 7\n\t"
      "s_add_u32 m0, s57, 14336\n\t"
      "s_add_u32 s54, s59, s15\n\t"
      "buffer_load_dwordx4 v98, s[24:27], s54 offen lds\n\t"
      "s_add_u32 m0, s57, 15360\n\t"
      "s_nop 0\n\t"
      "buffer_load_dwordx4 v99, s[24:27], s54 offen lds\n\t"
      "s_mov_b32 m0, s55\n\t"
      "s_add_u32 s54, s60, s15\n\t"
      "buffer_load_dwordx4 v100, s[28:31], s54 offen lds\n\t"
      "s_add_u32 m0, s55, 1024\n\t"
      "s_add_u32 s55, s55, s74\n\t"
      "buffer_load_dwordx4 v101, s[28:31], s54 offen lds\n\t"
      "s_add_u32 s15, s19, 2\n\t"
      "s_sub_u32 s14, s15, 7\n\t"
      "s_cmp_lt_u32 s15, 7\n\t"
      "s_cselect_b32 s15, s15, s14\n\t"
      "s_lshl_b32 s15, s15, 7\n\t"
      "s_add_u32 m0, s57, 28672\n\t"
      "s_add_u32 s54, s59, s15\n\t"
      "buffer_load_dwordx4 v98, s[24:27], s54 offen lds\n\t"
      "s_add_u32 m0, s57, 29696\n\t"
      "s_nop 0\n\t"
      "buffer_load_dwordx4 v99, s[24:27], s54 offen lds\n\t"
      "s_mov_b32 m0, s55\n\t"
      "s_add_u32 s54, s60, s15\n\t"
      "buffer_load_dwordx4 v100, s[28:31], s54 offen lds\n\t"
      "s_add_u32 m0, s55, 1024\n\t"
      "s_add_u32 s55, s55, s74\n\t"
      "buffer_load_dwordx4 v101, s[28:31], s54 offen lds\n\t"
      "s_add_u32 s15, s19, 3\n\t"
      "s_sub_u32 s14, s15, 7\n\t"
      "s_cmp_lt_u32 s15, 7\n\t"
      "s_cselect_b32 s15, s15, s14\n\t"
      "s_lshl_b32 s15, s15, 7\n\t"
      "s_add_u32 m0, s57, 43008\n\t"
      "s_add_u32 s54, s59, s15\n\t"
      "buffer_load_dwordx4 v98, s[24:27], s54 offen lds\n\t"
      "s_add_u32 m0, s57, 44032\n\t"
      "s_nop 0\n\t"
      "buffer_load_dwordx4 v99, s[24:27], s54 offen lds\n\t"
      "s_mov_b32 m0, s55\n\t"
      "s_add_u32 s54, s60, s15\n\t"
      "buffer_load_dwordx4 v100, s[28:31], s54 offen lds\n\t"
      "s_add_u32 m0, s55, 1024\n\t"
      "s_add_u32 s55, s55, s74\n\t"
      "buffer_load_dwordx4 v101, s[28:31], s54 offen lds\n\t"
      "s_branch L_p1lo_%=\n\t"
      "L_isshi_%=:\n\t"
      "s_mul_i32 s58, s56, 14336\n\t"
      "s_add_u32 s58, s58, 100352\n\t"
      "s_lshl_b32 s15, s19, 7\n\t"
      "s_add_u32 m0, s58, 0\n\t"
      "s_nop 0\n\t"
      "buffer_load_dwordx4 v96, s[20:23], s15 offen lds\n\t"
      "s_add_u32 m0, s58, 1024\n\t"
      "s_nop 0\n\t"
      "buffer_load_dwordx4 v97, s[20:23], s15 offen lds\n\t"
      "s_add_u32 s15, s15, 128\n\t"
      "s_cmp_eq_u32 s15, 896\n\t"
      "s_cselect_b32 s15, 0, s15\n\t"
      "s_add_u32 m0, s58, 2048\n\t"
      "s_nop 0\n\t"
      "buffer_load_dwordx4 v96, s[20:23], s15 offen lds\n\t"
      "s_add_u32 m0, s58, 3072\n\t"
      "s_nop 0\n\t"
      "buffer_load_dwordx4 v97, s[20:23], s15 offen lds\n\t"
      "s_add_u32 s15, s15, 128\n\t"
      "s_cmp_eq_u32 s15, 896\n\t"
      "s_cselect_b32 s15, 0, s15\n\t"
      "s_add_u32 m0, s58, 4096\n\t"
      "s_nop 0\n\t"
      "buffer_load_dwordx4 v96, s[20:23], s15 offen lds\n\t"
      "s_add_u32 m0, s58, 5120\n\t"
      "s_nop 0\n\t"
      "buffer_load_dwordx4 v97, s[20:23], s15 offen lds\n\t"
      "s_add_u32 s15, s15, 128\n\t"
      "s_cmp_eq_u32 s15, 896\n\t"
      "s_cselect_b32 s15, 0, s15\n\t"
      "s_add_u32 m0, s58, 6144\n\t"
      "s_nop 0\n\t"
      "buffer_load_dwordx4 v96, s[20:23], s15 offen lds\n\t"
      "s_add_u32 m0, s58, 7168\n\t"
      "s_nop 0\n\t"
      "buffer_load_dwordx4 v97, s[20:23], s15 offen lds\n\t"
      "s_add_u32 s15, s15, 128\n\t"
      "s_cmp_eq_u32 s15, 896\n\t"
      "s_cselect_b32 s15, 0, s15\n\t"
      "s_add_u32 m0, s58, 8192\n\t"
      "s_nop 0\n\t"
      "buffer_load_dwordx4 v96, s[20:23], s15 offen lds\n\t"
      "s_add_u32 m0, s58, 9216\n\t"
      "s_nop 0\n\t"
      "buffer_load_dwordx4 v97, s[20:23], s15 offen lds\n\t"
      "s_add_u32 s15, s15, 128\n\t"
      "s_cmp_eq_u32 s15, 896\n\t"
      "s_cselect_b32 s15, 0, s15\n\t"
      "s_add_u32 m0, s58, 10240\n\t"
      "s_nop 0\n\t"
      "buffer_load_dwordx4 v96, s[20:23], s15 offen lds\n\t"
      "s_add_u32 m0, s58, 11264\n\t"
      "s_nop 0\n\t"
      "buffer_load_dwordx4 v97, s[20:23], s15 offen lds\n\t"
      "s_add_u32 s15, s15, 128\n\t"
      "s_cmp_eq_u32 s15, 896\n\t"
      "s_cselect_b32 s15, 0, s15\n\t"
      "s_add_u32 m0, s58, 12288\n\t"
      "s_nop 0\n\t"
      "buffer_load_dwordx4 v96, s[20:23], s15 offen lds\n\t"
      "s_add_u32 m0, s58, 13312\n\t"
      "s_nop 0\n\t"
      "buffer_load_dwordx4 v97, s[20:23], s15 offen lds\n\t"
      "s_barrier\n\t"
      "s_lshl_b32 s54, s56, 2\n\t"
      "s_add_u32 s54, s54, 160768\n\t"
      "v_mov_b32 v102, s54\n\t"
      "v_mov_b32 v60, 0x3f803f80\n\t"
      "v_mov_b32 v61, 0x3f803f80\n\t"
      "v_mov_b32 v62, 0x3f803f80\n\t"
      "v_mov_b32 v63, 0x3f803f80\n\t"
      "s_waitcnt lgkmcnt(0)\n\t"
      "s_add_u32 s44, s44, 1\n\t"
      "s_addc_u32 s45, s45, 0\n\t"
      "s_waitcnt vmcnt(0)\n\t"
      "s_mov_b64 exec, 1\n\t"
      "ds_add_u32 v102, v126 offset:0\n\t"
      "s_mov_b64 exec, -1\n\t"
      "s_add_u32 s55, s57, 65536\n\t"
      "s_cmp_eq_u32 s56, 3\n\t"
      "s_cselect_b32 s55, 157696, s55\n\t"
      "s_add_u32 s15, s19, 4\n\t"
      "s_sub_u32 s14, s15, 7\n\t"
      "s_cmp_lt_u32 s15, 7\n\t"
      "s_cselect_b32 s15, s15, s14\n\t"
      "s_lshl_b32 s15, s15, 7\n\t"
      "s_add_u32 m0, s57, 57344\n\t"
      "s_add_u32 s54, s59, s15\n\t"
      "buffer_load_dwordx4 v98, s[24:27], s54 offen lds\n\t"
      "s_add_u32 m0, s57, 58368\n\t"
      "s_nop 0\n\t"
      "buffer_load_dwordx4 v99, s[24:27], s54 offen lds\n\t"
      "s_mov_b32 m0, s55\n\t"
      "s_add_u32 s54, s60, s15\n\t"
      "buffer_load_dwordx4 v100, s[28:31], s54 offen lds\n\t"
      "s_add_u32 m0, s55, 1024\n\t"
      "s_add_u32 s55, s55, s74\n\t"
      "buffer_load_dwordx4 v101, s[28:31], s54 offen lds\n\t"
      "s_add_u32 s15, s19, 5\n\t"
      "s_sub_u32 s14, s15, 7\n\t"
      "s_cmp_lt_u32 s15, 7\n\t"
      "s_cselect_b32 s15, s15, s14\n\t"
      "s_lshl_b32 s15, s15, 7\n\t"
      "s_add_u32 m0, s57, 71680\n\t"
      "s_add_u32 s54, s59, s15\n\t"
      "buffer_load_dwordx4 v98, s[24:27], s54 offen lds\n\t"
      "s_add_u32 m0, s57, 72704\n\t"
      "s_nop 0\n\t"
      "buffer_load_dwordx4 v99, s[24:27], s54 offen lds\n\t"
      "s_mov_b32 m0, s55\n\t"
      "s_add_u32 s54, s60, s15\n\t"
      "buffer_load_dwordx4 v100, s[28:31], s54 offen lds\n\t"
      "s_add_u32 m0, s55, 1024\n\t"
      "s_add_u32 s55, s55, s74\n\t"
      "buffer_load_dwordx4 v101, s[28:31], s54 offen lds\n\t"
      "s_add_u32 s15, s19, 6\n\t"
      "s_sub_u32 s14, s15, 7\n\t"
      "s_cmp_lt_u32 s15, 7\n\t"
      "s_cselect_b32 s15, s15, s14\n\t"
      "s_lshl_b32 s15, s15, 7\n\t"
      "s_add_u32 m0, s57, 86016\n\t"
      "s_add_u32 s54, s59, s15\n\t"
      "buffer_load_dwordx4 v98, s[24:27], s54 offen lds\n\t"
      "s_add_u32 m0, s57, 87040\n\t"
      "s_nop 0\n\t"
      "buffer_load_dwordx4 v99, s[24:27], s54 offen lds\n\t"
      "s_mov_b32 m0, s55\n\t"
      "s_add_u32 s54, s60, s15\n\t"
      "buffer_load_dwordx4 v100, s[28:31], s54 offen lds\n\t"
      "s_add_u32 m0, s55, 1024\n\t"
      "s_add_u32 s55, s55, s74\n\t"
      "buffer_load_dwordx4 v101, s[28:31], s54 offen lds\n\t"
      "s_mov_b32 s59, s56\n\t"
      "s_mov_b32 s60, s57\n\t"
      "s_and_b32 s54, s51, 7\n\t"
      "s_lshl_b32 s55, s52, 1\n\t"
      "s_add_u32 s54, s54, s55\n\t"
      "s_and_b32 s54, s54, 7\n\t"
      "s_lshl_b32 s18, s54, 1\n\t"
      "s_and_b32 s54, s40, 3\n\t"
      "s_add_u32 s16, s18, s54\n\t"
      "s_lshr_b32 s54, s54, 1\n\t"
      "s_add_u32 s17, s18, s54\n\t"
      "s_and_b32 s54, s40, 3\n\t"
      "s_lshl_b32 s54, s54, 5\n\t"
      "s_lshl_b32 s55, s51, 7\n\t"
      "s_add_u32 s54, s54, s55\n\t"
      "s_lshr_b32 s56, s40, 2\n\t"
      "s_lshl_b32 s55, s50, 11\n\t"
      "s_lshl_b32 s57, s56, 10\n\t"
      "s_add_u32 s55, s55, s57\n\t"
      "v_add_u32 v116, s54, v118\n\t"
      "v_lshlrev_b32 v116, 12, v116\n\t"
      "v_add_u32 v116, s55, v116\n\t"
      "v_add_u32 v110, v116, v119\n\t"
      "v_add_u32 v111, v116, v120\n\t"
      "v_add_u32 v111, 0x8000, v111\n\t"
      "s_lshl_b32 s12, s56, 16\n\t"
      "s_xor_b32 s13, s12, 0x10000\n\t"
      "s_and_b32 s55, s40, 3\n\t"
      "s_mul_i32 s57, s55, 14336\n\t"
      "s_add_u32 s57, s57, 100352\n\t"
      "s_mul_i32 s55, s55, 20480\n\t"
      "s_cmp_lt_u32 s40, 4\n\t"
      "s_cselect_b32 s58, s55, s57\n\t"
      "v_add_u32 v122, s58, v124\n\t"
      "v_add_u32 v123, s58, v125\n\t"
      "s_lshl_b32 s57, s56, 4\n\t"
      "s_add_u32 s57, s57, s54\n\t"
      "v_add_u32 v116, s57, v105\n\t"
      "v_lshlrev_b32 v116, 10, v116\n\t"
      "v_lshl_add_u32 v112, v106, 4, v116\n\t"
      "v_lshl_add_u32 v113, v106, 3, v116\n\t"
      "s_lshl_b32 s35, s53, 21\n\t"
      "s_lshl_b32 s57, s52, 8\n\t"
      "s_add_u32 s35, s35, s57\n\t"
      "s_add_u32 s35, s35, 5242880\n\t"
      "s_lshl_b32 s57, s56, 13\n\t"
      "v_add_u32 v108, s57, v107\n\t"
      "v_add_u32 v109, 0x10000, v108\n\t"
      "s_and_b32 s55, s40, 3\n\t"
      "s_lshl_b32 s55, s55, 10\n\t"
      "s_add_u32 s34, s57, s55\n\t"
      "s_lshl_b32 s57, s40, 12\n\t"
      "s_add_u32 s57, s57, 98304\n\t"
      "v_add_u32 v114, s57, v107\n\t"
      "s_xor_b32 s57, s40, 4\n\t"
      "s_lshl_b32 s57, s57, 12\n\t"
      "s_add_u32 s57, s57, 98304\n\t"
      "v_add_u32 v115, s57, v107\n\t"
      "s_lshr_b32 s54, s42, 2\n\t"
      "s_lshl_b32 s54, s54, 3\n\t"
      "s_add_u32 s54, s54, s41\n\t"
      "s_lshl_b32 s54, s54, 2\n\t"
      "s_and_b32 s55, s42, 3\n\t"
      "s_add_u32 s54, s54, s55\n\t"
      "s_lshl_b32 s10, s54, 5\n\t"
      "s_cmp_lt_u32 s41, 4\n\t"
      "s_cselect_b32 s54, 16, 12\n\t"
      "s_cmp_lt_u32 s42, s54\n\t"
      "s_cselect_b32 s10, s10, -1\n\t"
      "s_sub_u32 s54, s42, 12\n\t"
      "s_lshr_b32 s54, s54, 2\n\t"
      "s_lshl_b32 s54, s54, 2\n\t"
      "s_add_u32 s54, s54, s41\n\t"
      "s_sub_u32 s54, s54, 4\n\t"
      "s_lshl_b32 s11, s54, 2\n\t"
      "s_add_u32 s11, s11, s55\n\t"
      "s_lshl_b32 s11, s11, 5\n\t"
      "s_cmp_lt_u32 s54, 7\n\t"
      "s_cselect_b32 s11, s11, -1\n\t"
      "s_cmp_ge_u32 s41, 4\n\t"
      "s_cselect_b32 s11, s11, -1\n\t"
      "s_sub_u32 s54, s42, 12\n\t"
      "s_cmp_lt_u32 s54, 8\n\t"
      "s_cselect_b32 s11, s11, -1\n\t"
      "s_lshl_b32 s54, s49, 6\n\t"
      "s_lshl_b32 s55, s50, 5\n\t"
      "s_add_u32 s54, s54, s55\n\t"
      "s_lshl_b32 s55, s56, 4\n\t"
      "s_add_u32 s54, s54, s55\n\t"
      "s_mul_i32 s54, s54, 28\n\t"
      "s_mul_i32 s55, s52, 7\n\t"
      "s_add_u32 s54, s54, s55\n\t"
      "s_lshl_b32 s54, s54, 10\n\t"
      "s_add_u32 s54, s54, 1048576\n\t"
      "s_and_b32 s55, s40, 3\n\t"
      "s_lshl_b32 s57, s55, 10\n\t"
      "s_add_u32 s61, s54, s57\n\t"
      "s_mov_b32 s62, s54\n\t"
      "s_and_b32 s57, s55, 1\n\t"
      "s_lshl_b32 s57, s57, 10\n\t"
      "s_add_u32 s63, s54, s57\n\t"
      "v_add_u32 v116, s60, v124\n\t"
      "v_add_u32 v117, s60, v125\n\t"
      "v_add_u32 v118, 0x10000, v116\n\t"
      "v_add_u32 v119, 0x10000, v117\n\t"
      "s_waitcnt vmcnt(8)\n\t"
      "s_mov_b64 exec, 1\n\t"
      "ds_add_u32 v127, v126 offset:52\n\t"
      "s_mov_b64 exec, -1\n\t"
      "s_waitcnt vmcnt(4)\n\t"
      "s_mov_b64 exec, 1\n\t"
      "ds_add_u32 v127, v126 offset:56\n\t"
      "s_mov_b64 exec, -1\n\t"
      "s_waitcnt vmcnt(0)\n\t"
      "s_mov_b64 exec, 1\n\t"
      "ds_add_u32 v127, v126 offset:60\n\t"
      "s_mov_b64 exec, -1\n\t"
      "ds_read_b32 v92, v102 offset:16\n\t"
      "s_waitcnt lgkmcnt(0)\n\t"
      "v_readfirstlane_b32 s74, v92\n\t"
      "s_mov_b32 s14, 0\n\t"
      "s_cmp_eq_u32 s74, 2\n\t"
      "s_cbranch_scc1 L_spin1x_%=\n\t"
      "L_spin1_%=:\n\t"
      "ds_read_b32 v92, v102 offset:16\n\t"
      "s_waitcnt lgkmcnt(0)\n\t"
      "s_add_u32 s14, s14, 1\n\t"
      "v_readfirstlane_b32 s74, v92\n\t"
      "s_cmp_lt_u32 s14, 0x100000\n\t"
      "s_cbranch_scc0 L_spin1x_%=\n\t"
      "s_cmp_eq_u32 s74, 2\n\t"
      "s_cbranch_scc0 L_spin1_%=\n\t"
      "L_spin1x_%=:\n\t"
      "s_add_u32 s15, s18, 0\n\t"
      "s_and_b32 s15, s15, 15\n\t"
      "s_lshl_b32 s15, s15, 6\n\t"
      "s_add_u32 s15, s15, s12\n\t"
      "s_add_u32 m0, s58, 0\n\t"
      "s_nop 0\n\t"
      "buffer_load_dwordx4 v110, s[36:39], s15 offen lds\n\t"
      "s_add_u32 m0, s58, 1024\n\t"
      "s_nop 0\n\t"
      "buffer_load_dwordx4 v111, s[36:39], s15 offen lds\n\t"
      "s_add_u32 s15, s18, 0\n\t"
      "s_and_b32 s15, s15, 15\n\t"
      "s_lshl_b32 s15, s15, 6\n\t"
      "s_add_u32 s15, s15, s13\n\t"
      "s_add_u32 m0, s58, 2048\n\t"
      "s_nop 0\n\t"
      "buffer_load_dwordx4 v110, s[36:39], s15 offen lds\n\t"
      "s_add_u32 m0, s58, 3072\n\t"
      "s_nop 0\n\t"
      "buffer_load_dwordx4 v111, s[36:39], s15 offen lds\n\t"
      "s_add_u32 s15, s18, 2\n\t"
      "s_and_b32 s15, s15, 15\n\t"
      "s_lshl_b32 s15, s15, 6\n\t"
      "s_add_u32 s15, s15, s12\n\t"
      "s_add_u32 m0, s58, 4096\n\t"
      "s_nop 0\n\t"
      "buffer_load_dwordx4 v110, s[36:39], s15 offen lds\n\t"
      "s_add_u32 m0, s58, 5120\n\t"
      "s_nop 0\n\t"
      "buffer_load_dwordx4 v111, s[36:39], s15 offen lds\n\t"
      "s_add_u32 s15, s18, 2\n\t"
      "s_and_b32 s15, s15, 15\n\t"
      "s_lshl_b32 s15, s15, 6\n\t"
      "s_add_u32 s15, s15, s13\n\t"
      "s_add_u32 m0, s58, 6144\n\t"
      "s_nop 0\n\t"
      "buffer_load_dwordx4 v110, s[36:39], s15 offen lds\n\t"
      "s_add_u32 m0, s58, 7168\n\t"
      "s_nop 0\n\t"
      "buffer_load_dwordx4 v111, s[36:39], s15 offen lds\n\t"
      "s_lshl_b32 s15, s18, 6\n\t"
      "v_lshrrev_b32 v97, 1, v104\n\t"
      "v_and_b32 v98, 1, v104\n\t"
      "s_and_b32 s54, s40, 3\n\t"
      "s_lshl_b32 s54, s54, 5\n\t"
      "s_lshl_b32 s55, s51, 7\n\t"
      "s_add_u32 s54, s54, s55\n\t"
      "v_add_u32 v97, s54, v97\n\t"
      "v_lshlrev_b32 v97, 12, v97\n\t"
      "v_lshl_add_u32 v97, v98, 7, v97\n\t"
      "s_lshl_b32 s55, s50, 11\n\t"
      "s_add_u32 s15, s15, s55\n\t"
      "buffer_load_dword v103, v97, s[36:39], s15 offen\n\t"
      "ds_read_b128 v[0:3], v116 offset:57344\n\t"
      "ds_read_b128 v[4:7], v118 offset:0\n\t"
      "ds_read_b128 v[8:11], v117 offset:57344\n\t"
      "ds_read_b128 v[12:15], v119 offset:0\n\t"
      "ds_read_b128 v[16:19], v118 offset:6144\n\t"
      "ds_read_b128 v[20:23], v118 offset:14336\n\t"
      "ds_read_b128 v[24:27], v119 offset:6144\n\t"
      "ds_read_b128 v[28:31], v119 offset:14336\n\t"
      "s_waitcnt lgkmcnt(4)\n\t"
      "v_mfma_f32_16x16x32_bf16 v[76:79], v[60:63], v[0:3], 0\n\t"
      "v_mfma_f32_16x16x32_bf16 v[244:247], v[60:63], v[4:7], 0\n\t"
      "v_mfma_f32_16x16x32_bf16 v[76:79], v[60:63], v[8:11], v[76:79]\n\t"
      "v_mfma_f32_16x16x32_bf16 v[244:247], v[60:63], v[12:15], v[244:247]\n\t"
      "ds_read_b128 v[0:3], v118 offset:20480\n\t"
      "ds_read_b128 v[4:7], v118 offset:28672\n\t"
      "ds_read_b128 v[8:11], v119 offset:20480\n\t"
      "ds_read_b128 v[12:15], v119 offset:28672\n\t"
      "s_waitcnt lgkmcnt(4)\n\t"
      "v_mfma_f32_16x16x32_bf16 v[76:79], v[60:63], v[16:19], v[76:79]\n\t"
      "v_mfma_f32_16x16x32_bf16 v[244:247], v[60:63], v[20:23], v[244:247]\n\t"
      "v_mfma_f32_16x16x32_bf16 v[76:79], v[60:63], v[24:27], v[76:79]\n\t"
      "v_mfma_f32_16x16x32_bf16 v[244:247], v[60:63], v[28:31], v[244:247]\n\t"
      "ds_read_b32 v92, v127 offset:36\n\t"
      "s_waitcnt lgkmcnt(0)\n\t"
      "v_readfirstlane_b32 s74, v92\n\t"
      "s_mov_b32 s14, 0\n\t"
      "s_cmp_eq_u32 s74, 4\n\t"
      "s_cbranch_scc1 L_spin2x_%=\n\t"
      "L_spin2_%=:\n\t"
      "ds_read_b32 v92, v127 offset:36\n\t"
      "s_waitcnt lgkmcnt(0)\n\t"
      "s_add_u32 s14, s14, 1\n\t"
      "v_readfirstlane_b32 s74, v92\n\t"
      "s_cmp_lt_u32 s14, 0x100000\n\t"
      "s_cbranch_scc0 L_spin2x_%=\n\t"
      "s_cmp_eq_u32 s74, 4\n\t"
      "s_cbranch_scc0 L_spin2_%=\n\t"
      "L_spin2x_%=:\n\t"
      "ds_read_b128 v[16:19], v116 offset:0\n\t"
      "ds_read_b128 v[20:23], v116 offset:8192\n\t"
      "ds_read_b128 v[24:27], v117 offset:0\n\t"
      "ds_read_b128 v[28:31], v117 offset:8192\n\t"
      "s_waitcnt lgkmcnt(4)\n\t"
      "v_mfma_f32_16x16x32_bf16 v[76:79], v[60:63], v[0:3], v[76:79]\n\t"
      "v_mfma_f32_16x16x32_bf16 v[244:247], v[60:63], v[4:7], v[244:247]\n\t"
      "v_mfma_f32_16x16x32_bf16 v[76:79], v[60:63], v[8:11], v[76:79]\n\t"
      "v_mfma_f32_16x16x32_bf16 v[244:247], v[60:63], v[12:15], v[244:247]\n\t"
      "ds_read_b32 v92, v127 offset:40\n\t"
      "s_waitcnt lgkmcnt(0)\n\t"
      "v_readfirstlane_b32 s74, v92\n\t"
      "s_mov_b32 s14, 0\n\t"
      "s_cmp_eq_u32 s74, 4\n\t"
      "s_cbranch_scc1 L_spin3x_%=\n\t"
      "L_spin3_%=:\n\t"
      "ds_read_b32 v92, v127 offset:40\n\t"
      "s_waitcnt lgkmcnt(0)\n\t"
      "s_add_u32 s14, s14, 1\n\t"
      "v_readfirstlane_b32 s74, v92\n\t"
      "s_cmp_lt_u32 s14, 0x100000\n\t"
      "s_cbranch_scc0 L_spin3x_%=\n\t"
      "s_cmp_eq_u32 s74, 4\n\t"
      "s_cbranch_scc0 L_spin3_%=\n\t"
      "L_spin3x_%=:\n\t"
      "ds_read_b128 v[0:3], v116 offset:14336\n\t"
      "ds_read_b128 v[4:7], v116 offset:22528\n\t"
      "ds_read_b128 v[8:11], v117 offset:14336\n\t"
      "ds_read_b128 v[12:15], v117 offset:22528\n\t"
      "s_waitcnt lgkmcnt(4)\n\t"
      "v_mfma_f32_16x16x32_bf16 v[76:79], v[60:63], v[16:19], v[76:79]\n\t"
      "v_mfma_f32_16x16x32_bf16 v[244:247], v[60:63], v[20:23], v[244:247]\n\t"
      "v_mfma_f32_16x16x32_bf16 v[76:79], v[60:63], v[24:27], v[76:79]\n\t"
      "v_mfma_f32_16x16x32_bf16 v[244:247], v[60:63], v[28:31], v[244:247]\n\t"
      "ds_read_b32 v92, v127 offset:44\n\t"
      "s_waitcnt lgkmcnt(0)\n\t"
      "v_readfirstlane_b32 s74, v92\n\t"
      "s_mov_b32 s14, 0\n\t"
      "s_cmp_eq_u32 s74, 4\n\t"
      "s_cbranch_scc1 L_spin4x_%=\n\t"
      "L_spin4_%=:\n\t"
      "ds_read_b32 v92, v127 offset:44\n\t"
      "s_waitcnt lgkmcnt(0)\n\t"
      "s_add_u32 s14, s14, 1\n\t"
      "v_readfirstlane_b32 s74, v92\n\t"
      "s_cmp_lt_u32 s14, 0x100000\n\t"
      "s_cbranch_scc0 L_spin4x_%=\n\t"
      "s_cmp_eq_u32 s74, 4\n\t"
      "s_cbranch_scc0 L_spin4_%=\n\t"
      "L_spin4x_%=:\n\t"
      "ds_read_b128 v[16:19], v116 offset:28672\n\t"
      "ds_read_b128 v[20:23], v116 offset:36864\n\t"
      "ds_read_b128 v[24:27], v117 offset:28672\n\t"
      "ds_read_b128 v[28:31], v117 offset:36864\n\t"
      "s_waitcnt lgkmcnt(4)\n\t"
      "v_mfma_f32_16x16x32_bf16 v[76:79], v[60:63], v[0:3], v[76:79]\n\t"
      "v_mfma_f32_16x16x32_bf16 v[244:247], v[60:63], v[4:7], v[244:247]\n\t"
      "v_mfma_f32_16x16x32_bf16 v[76:79], v[60:63], v[8:11], v[76:79]\n\t"
      "v_mfma_f32_16x16x32_bf16 v[244:247], v[60:63], v[12:15], v[244:247]\n\t"
      "ds_read_b32 v92, v127 offset:48\n\t"
      "s_waitcnt lgkmcnt(0)\n\t"
      "v_readfirstlane_b32 s74, v92\n\t"
      "s_mov_b32 s14, 0\n\t"
      "s_cmp_eq_u32 s74, 4\n\t"
      "s_cbranch_scc1 L_spin5x_%=\n\t"
      "L_spin5_%=:\n\t"
      "ds_read_b32 v92, v127 offset:48\n\t"
      "s_waitcnt lgkmcnt(0)\n\t"
      "s_add_u32 s14, s14, 1\n\t"
      "v_readfirstlane_b32 s74, v92\n\t"
      "s_cmp_lt_u32 s14, 0x100000\n\t"
      "s_cbranch_scc0 L_spin5x_%=\n\t"
      "s_cmp_eq_u32 s74, 4\n\t"
      "s_cbranch_scc0 L_spin5_%=\n\t"
      "L_spin5x_%=:\n\t"
      "ds_read_b128 v[0:3], v116 offset:43008\n\t"
      "ds_read_b128 v[4:7], v116 offset:51200\n\t"
      "ds_read_b128 v[8:11], v117 offset:43008\n\t"
      "ds_read_b128 v[12:15], v117 offset:51200\n\t"
      "s_waitcnt lgkmcnt(4)\n\t"
      "v_mfma_f32_16x16x32_bf16 v[76:79], v[60:63], v[16:19], v[76:79]\n\t"
      "v_mfma_f32_16x16x32_bf16 v[244:247], v[60:63], v[20:23], v[244:247]\n\t"
      "v_mfma_f32_16x16x32_bf16 v[76:79], v[60:63], v[24:27], v[76:79]\n\t"
      "v_mfma_f32_16x16x32_bf16 v[244:247], v[60:63], v[28:31], v[244:247]\n\t"
      "s_waitcnt lgkmcnt(0)\n\t"
      "v_mfma_f32_16x16x32_bf16 v[76:79], v[60:63], v[0:3], v[76:79]\n\t"
      "v_mfma_f32_16x16x32_bf16 v[244:247], v[60:63], v[4:7], v[244:247]\n\t"
      "v_mfma_f32_16x16x32_bf16 v[76:79], v[60:63], v[8:11], v[76:79]\n\t"
      "v_mfma_f32_16x16x32_bf16 v[244:247], v[60:63], v[12:15], v[244:247]\n\t"
      "s_lshl_b32 s54, s59, 6\n\t"
      "s_add_u32 s54, s54, 159744\n\t"
      "v_lshlrev_b32 v116, 2, v105\n\t"
      "v_add_u32 v116, s54, v116\n\t"
      "s_nop 4\n\t"
      "ds_write_b32 v116, v76\n\t"
      "ds_write_b32 v116, v244 offset:256\n\t"
      "s_waitcnt lgkmcnt(0)\n\t"
      "s_mov_b64 exec, 1\n\t"
      "ds_add_u32 v127, v126 offset:64\n\t"
      "s_mov_b64 exec, -1\n\t"
      "s_add_u32 s15, s18, 4\n\t"
      "s_and_b32 s15, s15, 15\n\t"
      "s_lshl_b32 s15, s15, 6\n\t"
      "s_add_u32 s15, s15, s12\n\t"
      "s_add_u32 m0, s58, 8192\n\t"
      "s_nop 0\n\t"
      "buffer_load_dwordx4 v110, s[36:39], s15 offen lds\n\t"
      "s_add_u32 m0, s58, 9216\n\t"
      "s_nop 0\n\t"
      "buffer_load_dwordx4 v111, s[36:39], s15 offen lds\n\t"
      "s_add_u32 s15, s18, 4\n\t"
      "s_and_b32 s15, s15, 15\n\t"
      "s_lshl_b32 s15, s15, 6\n\t"
      "s_add_u32 s15, s15, s13\n\t"
      "s_add_u32 m0, s58, 10240\n\t"
      "s_nop 0\n\t"
      "buffer_load_dwordx4 v110, s[36:39], s15 offen lds\n\t"
      "s_add_u32 m0, s58, 11264\n\t"
      "s_nop 0\n\t"
      "buffer_load_dwordx4 v111, s[36:39], s15 offen lds\n\t"
      "s_waitcnt vmcnt(9)\n\t"
      "ds_read_b128 v[128:131], v122 offset:0\n\t"
      "ds_read_b128 v[136:139], v123 offset:0\n\t"
      "ds_read_b128 v[132:135], v122 offset:2048\n\t"
      "ds_read_b128 v[140:143], v123 offset:2048\n\t"
      "s_waitcnt lgkmcnt(0)\n\t"
      "s_add_u32 s15, s18, 6\n\t"
      "s_and_b32 s15, s15, 15\n\t"
      "s_lshl_b32 s15, s15, 6\n\t"
      "s_add_u32 s15, s15, s12\n\t"
      "s_add_u32 m0, s58, 0\n\t"
      "s_nop 0\n\t"
      "buffer_load_dwordx4 v110, s[36:39], s15 offen lds\n\t"
      "s_add_u32 m0, s58, 1024\n\t"
      "s_nop 0\n\t"
      "buffer_load_dwordx4 v111, s[36:39], s15 offen lds\n\t"
      "s_add_u32 s15, s18, 6\n\t"
      "s_and_b32 s15, s15, 15\n\t"
      "s_lshl_b32 s15, s15, 6\n\t"
      "s_add_u32 s15, s15, s13\n\t"
      "s_add_u32 m0, s58, 2048\n\t"
      "s_nop 0\n\t"
      "buffer_load_dwordx4 v110, s[36:39], s15 offen lds\n\t"
      "s_add_u32 m0, s58, 3072\n\t"
      "s_nop 0\n\t"
      "buffer_load_dwordx4 v111, s[36:39], s15 offen lds\n\t"
      "s_waitcnt vmcnt(9)\n\t"
      "ds_read_b128 v[144:147], v122 offset:4096\n\t"
      "ds_read_b128 v[152:155], v123 offset:4096\n\t"
      "ds_read_b128 v[148:151], v122 offset:6144\n\t"
      "ds_read_b128 v[156:159], v123 offset:6144\n\t"
      "s_waitcnt lgkmcnt(0)\n\t"
      "s_add_u32 s15, s18, 8\n\t"
      "s_and_b32 s15, s15, 15\n\t"
      "s_lshl_b32 s15, s15, 6\n\t"
      "s_add_u32 s15, s15, s12\n\t"
      "s_add_u32 m0, s58, 4096\n\t"
      "s_nop 0\n\t"
      "buffer_load_dwordx4 v110, s[36:39], s15 offen lds\n\t"
      "s_add_u32 m0, s58, 5120\n\t"
      "s_nop 0\n\t"
      "buffer_load_dwordx4 v111, s[36:39], s15 offen lds\n\t"
      "s_add_u32 s15, s18, 8\n\t"
      "s_and_b32 s15, s15, 15\n\t"
      "s_lshl_b32 s15, s15, 6\n\t"
      "s_add_u32 s15, s15, s13\n\t"
      "s_add_u32 m0, s58, 6144\n\t"
      "s_nop 0\n\t"
      "buffer_load_dwordx4 v110, s[36:39], s15 offen lds\n\t"
      "s_add_u32 m0, s58, 7168\n\t"
      "s_nop 0\n\t"
      "buffer_load_dwordx4 v111, s[36:39], s15 offen lds\n\t"
      "s_waitcnt vmcnt(8)\n\t"
      "ds_read_b128 v[160:163], v122 offset:8192\n\t"
      "ds_read_b128 v[168:171], v123 offset:8192\n\t"
      "ds_read_b128 v[164:167], v122 offset:10240\n\t"
      "ds_read_b128 v[172:175], v123 offset:10240\n\t"
      "s_waitcnt lgkmcnt(0)\n\t"
      "s_add_u32 s15, s18, 10\n\t"
      "s_and_b32 s15, s15, 15\n\t"
      "s_lshl_b32 s15, s15, 6\n\t"
      "s_add_u32 s15, s15, s12\n\t"
      "s_add_u32 m0, s58, 8192\n\t"
      "s_nop 0\n\t"
      "buffer_load_dwordx4 v110, s[36:39], s15 offen lds\n\t"
      "s_add_u32 m0, s58, 9216\n\t"
      "s_nop 0\n\t"
      "buffer_load_dwordx4 v111, s[36:39], s15 offen lds\n\t"
      "s_add_u32 s15, s18, 10\n\t"
      "s_and_b32 s15, s15, 15\n\t"
      "s_lshl_b32 s15, s15, 6\n\t"
      "s_add_u32 s15, s15, s13\n\t"
      "s_add_u32 m0, s58, 10240\n\t"
      "s_nop 0\n\t"
      "buffer_load_dwordx4 v110, s[36:39], s15 offen lds\n\t"
      "s_add_u32 m0, s58, 11264\n\t"
      "s_nop 0\n\t"
      "buffer_load_dwordx4 v111, s[36:39], s15 offen lds\n\t"
      "s_waitcnt vmcnt(8)\n\t"
      "ds_read_b128 v[176:179], v122 offset:0\n\t"
      "ds_read_b128 v[184:187], v123 offset:0\n\t"
      "ds_read_b128 v[180:183], v122 offset:2048\n\t"
      "ds_read_b128 v[188:191], v123 offset:2048\n\t"
      "s_waitcnt lgkmcnt(0)\n\t"
      "s_add_u32 s15, s18, 12\n\t"
      "s_and_b32 s15, s15, 15\n\t"
      "s_lshl_b32 s15, s15, 6\n\t"
      "s_add_u32 s15, s15, s12\n\t"
      "s_add_u32 m0, s58, 0\n\t"
      "s_nop 0\n\t"
      "buffer_load_dwordx4 v110, s[36:39], s15 offen lds\n\t"
      "s_add_u32 m0, s58, 1024\n\t"
      "s_nop 0\n\t"
      "buffer_load_dwordx4 v111, s[36:39], s15 offen lds\n\t"
      "s_add_u32 s15, s18, 12\n\t"
      "s_and_b32 s15, s15, 15\n\t"
      "s_lshl_b32 s15, s15, 6\n\t"
      "s_add_u32 s15, s15, s13\n\t"
      "s_add_u32 m0, s58, 2048\n\t"
      "s_nop 0\n\t"
      "buffer_load_dwordx4 v110, s[36:39], s15 offen lds\n\t"
      "s_add_u32 m0, s58, 3072\n\t"
      "s_nop 0\n\t"
      "buffer_load_dwordx4 v111, s[36:39], s15 offen lds\n\t"
      "s_waitcnt vmcnt(8)\n\t"
      "ds_read_b128 v[192:195], v122 offset:4096\n\t"
      "ds_read_b128 v[200:203], v123 offset:4096\n\t"
      "ds_read_b128 v[196:199], v122 offset:6144\n\t"
      "ds_read_b128 v[204:207], v123 offset:6144\n\t"
      "s_waitcnt lgkmcnt(0)\n\t"
      "s_add_u32 s15, s18, 14\n\t"
      "s_and_b32 s15, s15, 15\n\t"
      "s_lshl_b32 s15, s15, 6\n\t"
      "s_add_u32 s15, s15, s12\n\t"
      "s_add_u32 m0, s58, 4096\n\t"
      "s_nop 0\n\t"
      "buffer_load_dwordx4 v110, s[36:39], s15 offen lds\n\t"
      "s_add_u32 m0, s58, 5120\n\t"
      "s_nop 0\n\t"
      "buffer_load_dwordx4 v111, s[36:39], s15 offen lds\n\t"
      "s_add_u32 s15, s18, 14\n\t"
      "s_and_b32 s15, s15, 15\n\t"
      "s_lshl_b32 s15, s15, 6\n\t"
      "s_add_u32 s15, s15, s13\n\t"
      "s_add_u32 m0, s58, 6144\n\t"
      "s_nop 0\n\t"
      "buffer_load_dwordx4 v110, s[36:39], s15 offen lds\n\t"
      "s_add_u32 m0, s58, 7168\n\t"
      "s_nop 0\n\t"
      "buffer_load_dwordx4 v111, s[36:39], s15 offen lds\n\t"
      "s_waitcnt vmcnt(8)\n\t"
      "ds_read_b128 v[208:211], v122 offset:8192\n\t"
      "ds_read_b128 v[216:219], v123 offset:8192\n\t"
      "ds_read_b128 v[212:215], v122 offset:10240\n\t"
      "ds_read_b128 v[220:223], v123 offset:10240\n\t"
      "s_waitcnt vmcnt(4)\n\t"
      "ds_read_b128 v[224:227], v122 offset:0\n\t"
      "ds_read_b128 v[232:235], v123 offset:0\n\t"
      "ds_read_b128 v[228:231], v122 offset:2048\n\t"
      "ds_read_b128 v[236:239], v123 offset:2048\n\t"
      "s_waitcnt vmcnt(0)\n\t"
      "ds_read_b128 v[240:243], v122 offset:4096\n\t"
      "ds_read_b128 v[248:251], v123 offset:4096\n\t"
      "ds_read_b128 v[244:247], v122 offset:6144\n\t"
      "ds_read_b128 v[252:255], v123 offset:6144\n\t"
      "s_cmp_eq_u32 s40, 4\n\t"
      "s_cbranch_scc0 L_p1join_%=\n\t"
      "s_lshl_b32 s54, s50, 4\n\t"
      "v_add_u32 v118, s54, v105\n\t"
      "v_lshrrev_b32 v119, 3, v118\n\t"
      "v_lshlrev_b32 v119, 1, v119\n\t"
      "v_add_u32 v119, s49, v119\n\t"
      "v_and_b32 v118, 7, v118\n\t"
      "v_lshlrev_b32 v118, 2, v118\n\t"
      "v_add_u32 v118, s52, v118\n\t"
      "v_lshl_add_u32 v118, v118, 3, v119\n\t"
      "v_lshlrev_b32 v118, 7, v118\n\t"
      "v_lshl_add_u32 v118, v106, 3, v118\n\t"
      "s_mov_b32 s54, 4096\n\t"
      "s_mov_b32 s74, 0\n\t"
      "L_poll1_%=:\n\t"
      "buffer_load_dwordx2 v[120:121], v118, s[64:67], s54 offen sc1\n\t"
      "s_waitcnt vmcnt(0)\n\t"
      "v_cmp_ne_u32 vcc, s44, v120\n\t"
      "v_cmp_ne_u32 s[68:69], s45, v121\n\t"
      "s_nop 1\n\t"
      "s_or_b64 vcc, vcc, s[68:69]\n\t"
      "s_cbranch_scc0 L_p1join_%=\n\t"
      "s_sleep 1\n\t"
      "s_add_u32 s74, s74, 1\n\t"
      "s_cmp_lt_u32 s74, 0x40000\n\t"
      "s_cbranch_scc1 L_poll1_%=\n\t"
      "s_branch L_p1join_%=\n\t"
      "L_p1lo_%=:\n\t"
      "s_lshr_b32 s59, s40, 1\n\t"
      "s_and_b32 s60, s40, 1\n\t"
      "s_cmp_eq_u32 s40, 0\n\t"
      "s_cbranch_scc0 L_noinit_%=\n\t"
      "v_mov_b32 v92, 0\n\t"
      "v_lshl_add_u32 v93, v104, 2, v127\n\t"
      "ds_write_b32 v93, v92\n\t"
      "s_waitcnt lgkmcnt(0)\n\t"
      "L_noinit_%=:\n\t"
      "s_barrier\n\t"
      "s_and_b32 s54, s51, 7\n\t"
      "s_lshl_b32 s55, s52, 1\n\t"
      "s_add_u32 s54, s54, s55\n\t"
      "s_and_b32 s54, s54, 7\n\t"
      "s_lshl_b32 s18, s54, 1\n\t"
      "s_and_b32 s54, s40, 3\n\t"
      "s_add_u32 s16, s18, s54\n\t"
      "s_lshr_b32 s54, s54, 1\n\t"
      "s_add_u32 s17, s18, s54\n\t"
      "s_and_b32 s54, s40, 3\n\t"
      "s_lshl_b32 s54, s54, 5\n\t"
      "s_lshl_b32 s55, s51, 7\n\t"
      "s_add_u32 s54, s54, s55\n\t"
      "s_lshr_b32 s56, s40, 2\n\t"
      "s_lshl_b32 s55, s50, 11\n\t"
      "s_lshl_b32 s57, s56, 10\n\t"
      "s_add_u32 s55, s55, s57\n\t"
      "v_add_u32 v116, s54, v118\n\t"
      "v_lshlrev_b32 v116, 12, v116\n\t"
      "v_add_u32 v116, s55, v116\n\t"
      "v_add_u32 v110, v116, v119\n\t"
      "v_add_u32 v111, v116, v120\n\t"
      "v_add_u32 v111, 0x8000, v111\n\t"
      "s_lshl_b32 s12, s56, 16\n\t"
      "s_xor_b32 s13, s12, 0x10000\n\t"
      "s_and_b32 s55, s40, 3\n\t"
      "s_mul_i32 s57, s55, 14336\n\t"
      "s_add_u32 s57, s57, 100352\n\t"
      "s_mul_i32 s55, s55, 20480\n\t"
      "s_cmp_lt_u32 s40, 4\n\t"
      "s_cselect_b32 s58, s55, s57\n\t"
      "v_add_u32 v122, s58, v124\n\t"
      "v_add_u32 v123, s58, v125\n\t"
      "s_lshl_b32 s57, s56, 4\n\t"
      "s_add_u32 s57, s57, s54\n\t"
      "v_add_u32 v116, s57, v105\n\t"
      "v_lshlrev_b32 v116, 10, v116\n\t"
      "v_lshl_add_u32 v112, v106, 4, v116\n\t"
      "v_lshl_add_u32 v113, v106, 3, v116\n\t"
      "s_lshl_b32 s35, s53, 21\n\t"
      "s_lshl_b32 s57, s52, 8\n\t"
      "s_add_u32 s35, s35, s57\n\t"
      "s_add_u32 s35, s35, 5242880\n\t"
      "s_lshl_b32 s57, s56, 13\n\t"
      "v_add_u32 v108, s57, v107\n\t"
      "v_add_u32 v109, 0x10000, v108\n\t"
      "s_and_b32 s55, s40, 3\n\t"
      "s_lshl_b32 s55, s55, 10\n\t"
      "s_add_u32 s34, s57, s55\n\t"
      "s_lshl_b32 s57, s40, 12\n\t"
      "s_add_u32 s57, s57, 98304\n\t"
      "v_add_u32 v114, s57, v107\n\t"
      "s_xor_b32 s57, s40, 4\n\t"
      "s_lshl_b32 s57, s57, 12\n\t"
      "s_add_u32 s57, s57, 98304\n\t"
      "v_add_u32 v115, s57, v107\n\t"
      "s_lshr_b32 s54, s42, 2\n\t"
      "s_lshl_b32 s54, s54, 3\n\t"
      "s_add_u32 s54, s54, s41\n\t"
      "s_lshl_b32 s54, s54, 2\n\t"
      "s_and_b32 s55, s42, 3\n\t"
      "s_add_u32 s54, s54, s55\n\t"
      "s_lshl_b32 s10, s54, 5\n\t"
      "s_cmp_lt_u32 s41, 4\n\t"
      "s_cselect_b32 s54, 16, 12\n\t"
      "s_cmp_lt_u32 s42, s54\n\t"
      "s_cselect_b32 s10, s10, -1\n\t"
      "s_sub_u32 s54, s42, 12\n\t"
      "s_lshr_b32 s54, s54, 2\n\t"
      "s_lshl_b32 s54, s54, 2\n\t"
      "s_add_u32 s54, s54, s41\n\t"
      "s_sub_u32 s54, s54, 4\n\t"
      "s_lshl_b32 s11, s54, 2\n\t"
      "s_add_u32 s11, s11, s55\n\t"
      "s_lshl_b32 s11, s11, 5\n\t"
      "s_cmp_lt_u32 s54, 7\n\t"
      "s_cselect_b32 s11, s11, -1\n\t"
      "s_cmp_ge_u32 s41, 4\n\t"
      "s_cselect_b32 s11, s11, -1\n\t"
      "s_sub_u32 s54, s42, 12\n\t"
      "s_cmp_lt_u32 s54, 8\n\t"
      "s_cselect_b32 s11, s11, -1\n\t"
      "s_lshl_b32 s54, s49, 6\n\t"
      "s_lshl_b32 s55, s50, 5\n\t"
      "s_add_u32 s54, s54, s55\n\t"
      "s_lshl_b32 s55, s56, 4\n\t"
      "s_add_u32 s54, s54, s55\n\t"
      "s_mul_i32 s54, s54, 28\n\t"
      "s_mul_i32 s55, s52, 7\n\t"
      "s_add_u32 s54, s54, s55\n\t"
      "s_lshl_b32 s54, s54, 10\n\t"
      "s_add_u32 s54, s54, 1048576\n\t"
      "s_and_b32 s55, s40, 3\n\t"
      "s_lshl_b32 s57, s55, 10\n\t"
      "s_add_u32 s61, s54, s57\n\t"
      "s_mov_b32 s62, s54\n\t"
      "s_and_b32 s57, s55, 1\n\t"
      "s_lshl_b32 s57, s57, 10\n\t"
      "s_add_u32 s63, s54, s57\n\t"
      "s_mul_i32 s54, s59, 28672\n\t"
      "s_add_u32 s54, s54, 100352\n\t"
      "v_add_u32 v100, s54, v124\n\t"
      "v_add_u32 v101, s54, v125\n\t"
      "s_lshl_b32 s54, s60, 13\n\t"
      "v_add_u32 v96, s54, v124\n\t"
      "v_add_u32 v97, s54, v125\n\t"
      "v_add_u32 v98, 0xc000, v96\n\t"
      "v_add_u32 v99, 0xc000, v97\n\t"
      "s_lshl_b32 s54, s59, 3\n\t"
      "s_add_u32 s54, s54, 160768\n\t"
      "v_mov_b32 v121, s54\n\t"
      "s_lshl_b32 s54, s40, 2\n\t"
      "s_add_u32 s54, s54, 160768\n\t"
      "v_mov_b32 v120, s54\n\t"
      "s_lshl_b32 s54, s46, 6\n\t"
      "s_lshl_b32 s55, s48, 1\n\t"
      "s_add_u32 s54, s54, s55\n\t"
      "s_add_u32 s54, s54, s59\n\t"
      "s_mul_i32 s54, s54, 28\n\t"
      "s_mul_i32 s55, s47, 7\n\t"
      "s_add_u32 s54, s54, s55\n\t"
      "s_lshl_b32 s55, s60, 2\n\t"
      "s_add_u32 s54, s54, s55\n\t"
      "s_lshl_b32 s54, s54, 10\n\t"
      "s_add_u32 s75, s54, 1048576\n\t"
      "s_cmp_eq_u32 s60, 1\n\t"
      "s_cselect_b64 vcc, -1, 0\n\t"
      "s_waitcnt lgkmcnt(0)\n\t"
      "v_mov_b32 v240, 0x3f803f80\n\t"
      "v_mov_b32 v241, 0x3f803f80\n\t"
      "v_mov_b32 v242, 0x3f803f80\n\t"
      "v_mov_b32 v243, 0x3f803f80\n\t"
      "v_and_b32 v116, 1, v105\n\t"
      "v_cmp_ne_u32 s[68:69], 0, v116\n\t"
      "v_and_b32 v116, 2, v105\n\t"
      "v_cmp_ne_u32 s[70:71], 0, v116\n\t"
      "v_lshrrev_b32 v116, 2, v105\n\t"
      "v_cmp_eq_u32 s[72:73], v116, v106\n\t"
      "s_waitcnt lgkmcnt(0)\n\t"
      "s_add_u32 s44, s44, 1\n\t"
      "s_addc_u32 s45, s45, 0\n\t"
      "s_waitcnt vmcnt(12)\n\t"
      "s_mov_b64 exec, 1\n\t"
      "ds_add_u32 v127, v126 offset:36\n\t"
      "s_mov_b64 exec, -1\n\t"
      "s_waitcnt vmcnt(8)\n\t"
      "s_mov_b64 exec, 1\n\t"
      "ds_add_u32 v127, v126 offset:40\n\t"
      "s_mov_b64 exec, -1\n\t"
      "s_mov_b32 s14, 0\n\t"
      "L_xl_%=:\n\t"
      "ds_read_b64 v[92:93], v121 offset:0\n\t"
      "s_waitcnt lgkmcnt(0)\n\t"
      "v_and_b32 v92, v92, v93\n\t"
      "s_add_u32 s14, s14, 1\n\t"
      "v_readfirstlane_b32 s74, v92\n\t"
      "s_cmp_lt_u32 s14, 0x100000\n\t"
      "s_cbranch_scc0 L_xlx_%=\n\t"
      "s_cmp_eq_u32 s74, 1\n\t"
      "s_cbranch_scc0 L_xl_%=\n\t"
      "L_xlx_%=:\n\t"
      "ds_read_b128 v[128:131], v100 offset:0\n\t"
      "ds_read_b128 v[132:135], v101 offset:0\n\t"
      "ds_read_b128 v[136:139], v100 offset:2048\n\t"
      "ds_read_b128 v[140:143], v101 offset:2048\n\t"
      "ds_read_b128 v[144:147], v100 offset:4096\n\t"
      "ds_read_b128 v[148:151], v101 offset:4096\n\t"
      "ds_read_b128 v[152:155], v100 offset:6144\n\t"
      "ds_read_b128 v[156:159], v101 offset:6144\n\t"
      "ds_read_b128 v[160:163], v100 offset:8192\n\t"
      "ds_read_b128 v[164:167], v101 offset:8192\n\t"
      "ds_read_b128 v[168:171], v100 offset:10240\n\t"
      "ds_read_b128 v[172:175], v101 offset:10240\n\t"
      "ds_read_b128 v[176:179], v100 offset:12288\n\t"
      "ds_read_b128 v[180:183], v101 offset:12288\n\t"
      "ds_read_b128 v[184:187], v100 offset:14336\n\t"
      "ds_read_b128 v[188:191], v101 offset:14336\n\t"
      "ds_read_b128 v[192:195], v100 offset:16384\n\t"
      "ds_read_b128 v[196:199], v101 offset:16384\n\t"
      "ds_read_b128 v[200:203], v100 offset:18432\n\t"
      "ds_read_b128 v[204:207], v101 offset:18432\n\t"
      "ds_read_b128 v[208:211], v100 offset:20480\n\t"
      "ds_read_b128 v[212:215], v101 offset:20480\n\t"
      "ds_read_b128 v[216:219], v100 offset:22528\n\t"
      "ds_read_b128 v[220:223], v101 offset:22528\n\t"
      "ds_read_b128 v[224:227], v100 offset:24576\n\t"
      "ds_read_b128 v[228:231], v101 offset:24576\n\t"
      "ds_read_b128 v[232:235], v100 offset:26624\n\t"
      "ds_read_b128 v[236:239], v101 offset:26624\n\t"
      "ds_read_b32 v92, v127 offset:36\n\t"
      "s_waitcnt lgkmcnt(0)\n\t"
      "s_mov_b64 exec, 1\n\t"
      "ds_add_u32 v121, v126 offset:16\n\t"
      "s_mov_b64 exec, -1\n\t"
      "s_mov_b64 exec, 1\n\t"
      "ds_add_u32 v121, v126 offset:20\n\t"
      "s_mov_b64 exec, -1\n\t"
      "v_readfirstlane_b32 s74, v92\n\t"
      "s_mov_b32 s14, 0\n\t"
      "s_cmp_eq_u32 s74, 4\n\t"
      "s_cbranch_scc1 L_spin6x_%=\n\t"
      "L_spin6_%=:\n\t"
      "ds_read_b32 v92, v127 offset:36\n\t"
      "s_waitcnt lgkmcnt(0)\n\t"
      "s_add_u32 s14, s14, 1\n\t"
      "v_readfirstlane_b32 s74, v92\n\t"
      "s_cmp_lt_u32 s14, 0x100000\n\t"
      "s_cbranch_scc0 L_spin6x_%=\n\t"
      "s_cmp_eq_u32 s74, 4\n\t"
      "s_cbranch_scc0 L_spin6_%=\n\t"
      "L_spin6x_%=:\n\t"
      "ds_read_b128 v[32:35], v96 offset:0\n\t"
      "ds_read_b128 v[36:39], v96 offset:2048\n\t"
      "ds_read_b128 v[40:43], v96 offset:4096\n\t"
      "s_cbranch_vccnz L_r0_%=\n\t"
      "ds_read_b128 v[44:47], v96 offset:6144\n\t"
      "L_r0_%=:\n\t"
      "ds_read_b128 v[48:51], v97 offset:0\n\t"
      "ds_read_b128 v[52:55], v97 offset:2048\n\t"
      "ds_read_b128 v[56:59], v97 offset:4096\n\t"
      "s_cbranch_vccnz L_r1_%=\n\t"
      "ds_read_b128 v[60:63], v97 offset:6144\n\t"
      "L_r1_%=:\n\t"
      "ds_read_b32 v92, v127 offset:40\n\t"
      "s_cbranch_vccnz L_sth1_%=\n\t"
      "v_mfma_f32_16x16x32_bf16 v[244:247], v[128:131], v[240:243], 0\n\t"
      "v_mfma_f32_16x16x32_bf16 v[248:251], v[128:131], v[128:131], 0\n\t"
      "v_mfma_f32_16x16x32_bf16 v[244:247], v[132:135], v[240:243], v[244:247]\n\t"
      "v_mfma_f32_16x16x32_bf16 v[248:251], v[132:135], v[132:135], v[248:251]\n\t"
      "v_mfma_f32_16x16x32_bf16 v[244:247], v[136:139], v[240:243], v[244:247]\n\t"
      "v_mfma_f32_16x16x32_bf16 v[248:251], v[136:139], v[136:139], v[248:251]\n\t"
      "v_mfma_f32_16x16x32_bf16 v[244:247], v[140:143], v[240:243], v[244:247]\n\t"
      "v_mfma_f32_16x16x32_bf16 v[248:251], v[140:143], v[140:143], v[248:251]\n\t"
      "v_mfma_f32_16x16x32_bf16 v[244:247], v[144:147], v[240:243], v[244:247]\n\t"
      "v_mfma_f32_16x16x32_bf16 v[248:251], v[144:147], v[144:147], v[248:251]\n\t"
      "v_mfma_f32_16x16x32_bf16 v[244:247], v[148:151], v[240:243], v[244:247]\n\t"
      "v_mfma_f32_16x16x32_bf16 v[248:251], v[148:151], v[148:151], v[248:251]\n\t"
      "v_mfma_f32_16x16x32_bf16 v[244:247], v[152:155], v[240:243], v[244:247]\n\t"
      "v_mfma_f32_16x16x32_bf16 v[248:251], v[152:155], v[152:155], v[248:251]\n\t"
      "v_mfma_f32_16x16x32_bf16 v[244:247], v[156:159], v[240:243], v[244:247]\n\t"
      "v_mfma_f32_16x16x32_bf16 v[248:251], v[156:159], v[156:159], v[248:251]\n\t"
      "v_mfma_f32_16x16x32_bf16 v[244:247], v[160:163], v[240:243], v[244:247]\n\t"
      "v_mfma_f32_16x16x32_bf16 v[248:251], v[160:163], v[160:163], v[248:251]\n\t"
      "v_mfma_f32_16x16x32_bf16 v[244:247], v[164:167], v[240:243], v[244:247]\n\t"
      "v_mfma_f32_16x16x32_bf16 v[248:251], v[164:167], v[164:167], v[248:251]\n\t"
      "v_mfma_f32_16x16x32_bf16 v[244:247], v[168:171], v[240:243], v[244:247]\n\t"
      "v_mfma_f32_16x16x32_bf16 v[248:251], v[168:171], v[168:171], v[248:251]\n\t"
      "v_mfma_f32_16x16x32_bf16 v[244:247], v[172:175], v[240:243], v[244:247]\n\t"
      "v_mfma_f32_16x16x32_bf16 v[248:251], v[172:175], v[172:175], v[248:251]\n\t"
      "v_mfma_f32_16x16x32_bf16 v[244:247], v[176:179], v[240:243], v[244:247]\n\t"
      "v_mfma_f32_16x16x32_bf16 v[248:251], v[176:179], v[176:179], v[248:251]\n\t"
      "v_mfma_f32_16x16x32_bf16 v[244:247], v[180:183], v[240:243], v[244:247]\n\t"
      "v_mfma_f32_16x16x32_bf16 v[248:251], v[180:183], v[180:183], v[248:251]\n\t"
      "s_branch L_sthj_%=\n\t"
      "L_sth1_%=:\n\t"
      "v_mfma_f32_16x16x32_bf16 v[244:247], v[184:187], v[240:243], 0\n\t"
      "v_mfma_f32_16x16x32_bf16 v[248:251], v[184:187], v[184:187], 0\n\t"
      "v_mfma_f32_16x16x32_bf16 v[244:247], v[188:191], v[240:243], v[244:247]\n\t"
      "v_mfma_f32_16x16x32_bf16 v[248:251], v[188:191], v[188:191], v[248:251]\n\t"
      "v_mfma_f32_16x16x32_bf16 v[244:247], v[192:195], v[240:243], v[244:247]\n\t"
      "v_mfma_f32_16x16x32_bf16 v[248:251], v[192:195], v[192:195], v[248:251]\n\t"
      "v_mfma_f32_16x16x32_bf16 v[244:247], v[196:199], v[240:243], v[244:247]\n\t"
      "v_mfma_f32_16x16x32_bf16 v[248:251], v[196:199], v[196:199], v[248:251]\n\t"
      "v_mfma_f32_16x16x32_bf16 v[244:247], v[200:203], v[240:243], v[244:247]\n\t"
      "v_mfma_f32_16x16x32_bf16 v[248:251], v[200:203], v[200:203], v[248:251]\n\t"
      "v_mfma_f32_16x16x32_bf16 v[244:247], v[204:207], v[240:243], v[244:247]\n\t"
      "v_mfma_f32_16x16x32_bf16 v[248:251], v[204:207], v[204:207], v[248:251]\n\t"
      "v_mfma_f32_16x16x32_bf16 v[244:247], v[208:211], v[240:243], v[244:247]\n\t"
      "v_mfma_f32_16x16x32_bf16 v[248:251], v[208:211], v[208:211], v[248:251]\n\t"
      "v_mfma_f32_16x16x32_bf16 v[244:247], v[212:215], v[240:243], v[244:247]\n\t"
      "v_mfma_f32_16x16x32_bf16 v[248:251], v[212:215], v[212:215], v[248:251]\n\t"
      "v_mfma_f32_16x16x32_bf16 v[244:247], v[216:219], v[240:243], v[244:247]\n\t"
      "v_mfma_f32_16x16x32_bf16 v[248:251], v[216:219], v[216:219], v[248:251]\n\t"
      "v_mfma_f32_16x16x32_bf16 v[244:247], v[220:223], v[240:243], v[244:247]\n\t"
      "v_mfma_f32_16x16x32_bf16 v[248:251], v[220:223], v[220:223], v[248:251]\n\t"
      "v_mfma_f32_16x16x32_bf16 v[244:247], v[224:227], v[240:243], v[244:247]\n\t"
      "v_mfma_f32_16x16x32_bf16 v[248:251], v[224:227], v[224:227], v[248:251]\n\t"
      "v_mfma_f32_16x16x32_bf16 v[244:247], v[228:231], v[240:243], v[244:247]\n\t"
      "v_mfma_f32_16x16x32_bf16 v[248:251], v[228:231], v[228:231], v[248:251]\n\t"
      "v_mfma_f32_16x16x32_bf16 v[244:247], v[232:235], v[240:243], v[244:247]\n\t"
      "v_mfma_f32_16x16x32_bf16 v[248:251], v[232:235], v[232:235], v[248:251]\n\t"
      "v_mfma_f32_16x16x32_bf16 v[244:247], v[236:239], v[240:243], v[244:247]\n\t"
      "v_mfma_f32_16x16x32_bf16 v[248:251], v[236:239], v[236:239], v[248:251]\n\t"
      "L_sthj_%=:\n\t"
      "s_waitcnt lgkmcnt(0)\n\t"
      "s_waitcnt vmcnt(4)\n\t"
      "s_mov_b64 exec, 1\n\t"
      "ds_add_u32 v127, v126 offset:44\n\t"
      "s_mov_b64 exec, -1\n\t"
      "v_readfirstlane_b32 s74, v92\n\t"
      "s_mov_b32 s14, 0\n\t"
      "s_cmp_eq_u32 s74, 4\n\t"
      "s_cbranch_scc1 L_spin7x_%=\n\t"
      "L_spin7_%=:\n\t"
      "ds_read_b32 v92, v127 offset:40\n\t"
      "s_waitcnt lgkmcnt(0)\n\t"
      "s_add_u32 s14, s14, 1\n\t"
      "v_readfirstlane_b32 s74, v92\n\t"
      "s_cmp_lt_u32 s14, 0x100000\n\t"
      "s_cbranch_scc0 L_spin7x_%=\n\t"
      "s_cmp_eq_u32 s74, 4\n\t"
      "s_cbranch_scc0 L_spin7_%=\n\t"
      "L_spin7x_%=:\n\t"
      "ds_read_b32 v92, v127 offset:44\n\t"
      "v_mfma_f32_16x16x32_bf16 v[0:3], v[128:131], v[32:35], 0\n\t"
      "v_mfma_f32_16x16x32_bf16 v[4:7], v[184:187], v[32:35], 0\n\t"
      "ds_read_b128 v[32:35], v96 offset:14336\n\t"
      "v_mfma_f32_16x16x32_bf16 v[8:11], v[128:131], v[36:39], 0\n\t"
      "v_mfma_f32_16x16x32_bf16 v[12:15], v[184:187], v[36:39], 0\n\t"
      "ds_read_b128 v[36:39], v96 offset:16384\n\t"
      "v_mfma_f32_16x16x32_bf16 v[16:19], v[128:131], v[40:43], 0\n\t"
      "v_mfma_f32_16x16x32_bf16 v[20:23], v[184:187], v[40:43], 0\n\t"
      "ds_read_b128 v[40:43], v96 offset:18432\n\t"
      "s_cbranch_vccnz L_m0_%=\n\t"
      "v_mfma_f32_16x16x32_bf16 v[24:27], v[128:131], v[44:47], 0\n\t"
      "v_mfma_f32_16x16x32_bf16 v[28:31], v[184:187], v[44:47], 0\n\t"
      "ds_read_b128 v[44:47], v96 offset:20480\n\t"
      "L_m0_%=:\n\t"
      "v_mfma_f32_16x16x32_bf16 v[0:3], v[132:135], v[48:51], v[0:3]\n\t"
      "v_mfma_f32_16x16x32_bf16 v[4:7], v[188:191], v[48:51], v[4:7]\n\t"
      "ds_read_b128 v[48:51], v97 offset:14336\n\t"
      "v_mfma_f32_16x16x32_bf16 v[8:11], v[132:135], v[52:55], v[8:11]\n\t"
      "v_mfma_f32_16x16x32_bf16 v[12:15], v[188:191], v[52:55], v[12:15]\n\t"
      "ds_read_b128 v[52:55], v97 offset:16384\n\t"
      "v_mfma_f32_16x16x32_bf16 v[16:19], v[132:135], v[56:59], v[16:19]\n\t"
      "v_mfma_f32_16x16x32_bf16 v[20:23], v[188:191], v[56:59], v[20:23]\n\t"
      "ds_read_b128 v[56:59], v97 offset:18432\n\t"
      "s_cbranch_vccnz L_m1_%=\n\t"
      "v_mfma_f32_16x16x32_bf16 v[24:27], v[132:135], v[60:63], v[24:27]\n\t"
      "v_mfma_f32_16x16x32_bf16 v[28:31], v[188:191], v[60:63], v[28:31]\n\t"
      "ds_read_b128 v[60:63], v97 offset:20480\n\t"
      "L_m1_%=:\n\t"
      "v_cndmask_b32 v84, v248, v249, s[68:69]\n\t"
      "v_cndmask_b32 v85, v250, v251, s[68:69]\n\t"
      "v_cndmask_b32 v84, v84, v85, s[70:71]\n\t"
      "v_cndmask_b32 v84, 0, v84, s[72:73]\n\t"
      "v_mov_b32 v85, 1.0\n\t"
      "s_nop 1\n\t"
      "v_mfma_f32_16x16x4_f32 v[248:251], v84, v85, 0\n\t"
      "s_waitcnt lgkmcnt(3)\n\t"
      "s_waitcnt vmcnt(0)\n\t"
      "s_mov_b64 exec, 1\n\t"
      "ds_add_u32 v127, v126 offset:48\n\t"
      "s_mov_b64 exec, -1\n\t"
      "v_readfirstlane_b32 s74, v92\n\t"
      "s_mov_b32 s14, 0\n\t"
      "s_cmp_eq_u32 s74, 4\n\t"
      "s_cbranch_scc1 L_spin8x_%=\n\t"
      "L_spin8_%=:\n\t"
      "ds_read_b32 v92, v127 offset:44\n\t"
      "s_waitcnt lgkmcnt(0)\n\t"
      "s_add_u32 s14, s14, 1\n\t"
      "v_readfirstlane_b32 s74, v92\n\t"
      "s_cmp_lt_u32 s14, 0x100000\n\t"
      "s_cbranch_scc0 L_spin8x_%=\n\t"
      "s_cmp_eq_u32 s74, 4\n\t"
      "s_cbranch_scc0 L_spin8_%=\n\t"
      "L_spin8x_%=:\n\t"
      "ds_read_b32 v92, v127 offset:48\n\t"
      "v_mfma_f32_16x16x32_bf16 v[0:3], v[136:139], v[32:35], v[0:3]\n\t"
      "v_mfma_f32_16x16x32_bf16 v[4:7], v[192:195], v[32:35], v[4:7]\n\t"
      "ds_read_b128 v[32:35], v96 offset:28672\n\t"
      "v_mfma_f32_16x16x32_bf16 v[8:11], v[136:139], v[36:39], v[8:11]\n\t"
      "v_mfma_f32_16x16x32_bf16 v[12:15], v[192:195], v[36:39], v[12:15]\n\t"
      "ds_read_b128 v[36:39], v96 offset:30720\n\t"
      "v_mfma_f32_16x16x32_bf16 v[16:19], v[136:139], v[40:43], v[16:19]\n\t"
      "v_mfma_f32_16x16x32_bf16 v[20:23], v[192:195], v[40:43], v[20:23]\n\t"
      "ds_read_b128 v[40:43], v96 offset:32768\n\t"
      "s_cbranch_vccnz L_m2_%=\n\t"
      "v_mfma_f32_16x16x32_bf16 v[24:27], v[136:139], v[44:47], v[24:27]\n\t"
      "v_mfma_f32_16x16x32_bf16 v[28:31], v[192:195], v[44:47], v[28:31]\n\t"
      "ds_read_b128 v[44:47], v96 offset:34816\n\t"
      "L_m2_%=:\n\t"
      "s_waitcnt lgkmcnt(4)\n\t"
      "v_mfma_f32_16x16x32_bf16 v[0:3], v[140:143], v[48:51], v[0:3]\n\t"
      "v_mfma_f32_16x16x32_bf16 v[4:7], v[196:199], v[48:51], v[4:7]\n\t"
      "ds_read_b128 v[48:51], v97 offset:28672\n\t"
      "v_mfma_f32_16x16x32_bf16 v[8:11], v[140:143], v[52:55], v[8:11]\n\t"
      "v_mfma_f32_16x16x32_bf16 v[12:15], v[196:199], v[52:55], v[12:15]\n\t"
      "ds_read_b128 v[52:55], v97 offset:30720\n\t"
      "v_mfma_f32_16x16x32_bf16 v[16:19], v[140:143], v[56:59], v[16:19]\n\t"
      "v_mfma_f32_16x16x32_bf16 v[20:23], v[196:199], v[56:59], v[20:23]\n\t"
      "ds_read_b128 v[56:59], v97 offset:32768\n\t"
      "s_cbranch_vccnz L_m3_%=\n\t"
      "v_mfma_f32_16x16x32_bf16 v[24:27], v[140:143], v[60:63], v[24:27]\n\t"
      "v_mfma_f32_16x16x32_bf16 v[28:31], v[196:199], v[60:63], v[28:31]\n\t"
      "ds_read_b128 v[60:63], v97 offset:34816\n\t"
      "L_m3_%=:\n\t"
      "v_mul_f32 v64, 0x3b124925, v244\n\t"
      "v_mul_f32 v72, 0x3b124925, v248\n\t"
      "v_mul_f32 v65, 0x3b124925, v245\n\t"
      "v_mul_f32 v73, 0x3b124925, v249\n\t"
      "v_mul_f32 v66, 0x3b124925, v246\n\t"
      "v_mul_f32 v74, 0x3b124925, v250\n\t"
      "v_mul_f32 v67, 0x3b124925, v247\n\t"
      "v_mul_f32 v75, 0x3b124925, v251\n\t"
      "v_fma_f32 v72, -v64, v64, v72\n\t"
      "v_max_f32 v72, 0, v72\n\t"
      "v_fma_f32 v73, -v65, v65, v73\n\t"
      "v_max_f32 v73, 0, v73\n\t"
      "v_fma_f32 v74, -v66, v66, v74\n\t"
      "v_max_f32 v74, 0, v74\n\t"
      "v_fma_f32 v75, -v67, v67, v75\n\t"
      "v_max_f32 v75, 0, v75\n\t"
      "s_waitcnt lgkmcnt(3)\n\t"
      "v_readfirstlane_b32 s74, v92\n\t"
      "s_mov_b32 s14, 0\n\t"
      "s_cmp_eq_u32 s74, 4\n\t"
      "s_cbranch_scc1 L_spin9x_%=\n\t"
      "L_spin9_%=:\n\t"
      "ds_read_b32 v92, v127 offset:48\n\t"
      "s_waitcnt lgkmcnt(0)\n\t"
      "s_add_u32 s14, s14, 1\n\t"
      "v_readfirstlane_b32 s74, v92\n\t"
      "s_cmp_lt_u32 s14, 0x100000\n\t"
      "s_cbranch_scc0 L_spin9x_%=\n\t"
      "s_cmp_eq_u32 s74, 4\n\t"
      "s_cbranch_scc0 L_spin9_%=\n\t"
      "L_spin9x_%=:\n\t"
      "ds_read_b32 v92, v127 offset:52\n\t"
      "v_mfma_f32_16x16x32_bf16 v[0:3], v[144:147], v[32:35], v[0:3]\n\t"
      "v_mfma_f32_16x16x32_bf16 v[4:7], v[200:203], v[32:35], v[4:7]\n\t"
      "ds_read_b128 v[32:35], v96 offset:43008\n\t"
      "v_mfma_f32_16x16x32_bf16 v[8:11], v[144:147], v[36:39], v[8:11]\n\t"
      "v_mfma_f32_16x16x32_bf16 v[12:15], v[200:203], v[36:39], v[12:15]\n\t"
      "ds_read_b128 v[36:39], v96 offset:45056\n\t"
      "v_mfma_f32_16x16x32_bf16 v[16:19], v[144:147], v[40:43], v[16:19]\n\t"
      "v_mfma_f32_16x16x32_bf16 v[20:23], v[200:203], v[40:43], v[20:23]\n\t"
      "ds_read_b128 v[40:43], v96 offset:47104\n\t"
      "s_cbranch_vccnz L_m4_%=\n\t"
      "v_mfma_f32_16x16x32_bf16 v[24:27], v[144:147], v[44:47], v[24:27]\n\t"
      "v_mfma_f32_16x16x32_bf16 v[28:31], v[200:203], v[44:47], v[28:31]\n\t"
      "ds_read_b128 v[44:47], v98 offset:0\n\t"
      "L_m4_%=:\n\t"
      "v_cvt_pk_bf16_f32 v84, v64, v65\n\t"
      "v_cvt_pk_bf16_f32 v85, v66, v67\n\t"
      "v_lshlrev_b32 v64, 16, v84\n\t"
      "v_and_b32 v65, 0xffff0000, v84\n\t"
      "v_lshlrev_b32 v66, 16, v85\n\t"
      "v_and_b32 v67, 0xffff0000, v85\n\t"
      "v_cvt_pk_bf16_f32 v84, v72, v73\n\t"
      "v_cvt_pk_bf16_f32 v85, v74, v75\n\t"
      "v_lshlrev_b32 v72, 16, v84\n\t"
      "v_and_b32 v73, 0xffff0000, v84\n\t"
      "v_lshlrev_b32 v74, 16, v85\n\t"
      "v_and_b32 v75, 0xffff0000, v85\n\t"
      "s_waitcnt lgkmcnt(4)\n\t"
      "v_mfma_f32_16x16x32_bf16 v[0:3], v[148:151], v[48:51], v[0:3]\n\t"
      "v_mfma_f32_16x16x32_bf16 v[4:7], v[204:207], v[48:51], v[4:7]\n\t"
      "ds_read_b128 v[48:51], v97 offset:43008\n\t"
      "v_mfma_f32_16x16x32_bf16 v[8:11], v[148:151], v[52:55], v[8:11]\n\t"
      "v_mfma_f32_16x16x32_bf16 v[12:15], v[204:207], v[52:55], v[12:15]\n\t"
      "ds_read_b128 v[52:55], v97 offset:45056\n\t"
      "v_mfma_f32_16x16x32_bf16 v[16:19], v[148:151], v[56:59], v[16:19]\n\t"
      "v_mfma_f32_16x16x32_bf16 v[20:23], v[204:207], v[56:59], v[20:23]\n\t"
      "ds_read_b128 v[56:59], v97 offset:47104\n\t"
      "s_cbranch_vccnz L_m5_%=\n\t"
      "v_mfma_f32_16x16x32_bf16 v[24:27], v[148:151], v[60:63], v[24:27]\n\t"
      "v_mfma_f32_16x16x32_bf16 v[28:31], v[204:207], v[60:63], v[28:31]\n\t"
      "ds_read_b128 v[60:63], v99 offset:0\n\t"
      "L_m5_%=:\n\t"
      "v_add_f32 v72, 0x3727c5ac, v72\n\t"
      "v_add_f32 v73, 0x3727c5ac, v73\n\t"
      "v_add_f32 v74, 0x3727c5ac, v74\n\t"
      "v_add_f32 v75, 0x3727c5ac, v75\n\t"
      "v_cvt_pk_bf16_f32 v84, v72, v73\n\t"
      "v_cvt_pk_bf16_f32 v85, v74, v75\n\t"
      "v_lshlrev_b32 v72, 16, v84\n\t"
      "v_and_b32 v73, 0xffff0000, v84\n\t"
      "v_lshlrev_b32 v74, 16, v85\n\t"
      "v_and_b32 v75, 0xffff0000, v85\n\t"
      "v_sqrt_f32 v72, v72\n\t"
      "v_sqrt_f32 v73, v73\n\t"
      "v_sqrt_f32 v74, v74\n\t"
      "v_sqrt_f32 v75, v75\n\t"
      "s_waitcnt lgkmcnt(3)\n\t"
      "v_readfirstlane_b32 s74, v92\n\t"
      "s_mov_b32 s14, 0\n\t"
      "s_cmp_eq_u32 s74, 4\n\t"
      "s_cbranch_scc1 L_spin10x_%=\n\t"
      "L_spin10_%=:\n\t"
      "ds_read_b32 v92, v127 offset:52\n\t"
      "s_waitcnt lgkmcnt(0)\n\t"
      "s_add_u32 s14, s14, 1\n\t"
      "v_readfirstlane_b32 s74, v92\n\t"
      "s_cmp_lt_u32 s14, 0x100000\n\t"
      "s_cbranch_scc0 L_spin10x_%=\n\t"
      "s_cmp_eq_u32 s74, 4\n\t"
      "s_cbranch_scc0 L_spin10_%=\n\t"
      "L_spin10x_%=:\n\t"
      "ds_read_b32 v92, v127 offset:56\n\t"
      "v_mfma_f32_16x16x32_bf16 v[0:3], v[152:155], v[32:35], v[0:3]\n\t"
      "v_mfma_f32_16x16x32_bf16 v[4:7], v[208:211], v[32:35], v[4:7]\n\t"
      "ds_read_b128 v[32:35], v98 offset:8192\n\t"
      "v_mfma_f32_16x16x32_bf16 v[8:11], v[152:155], v[36:39], v[8:11]\n\t"
      "v_mfma_f32_16x16x32_bf16 v[12:15], v[208:211], v[36:39], v[12:15]\n\t"
      "ds_read_b128 v[36:39], v98 offset:10240\n\t"
      "v_mfma_f32_16x16x32_bf16 v[16:19], v[152:155], v[40:43], v[16:19]\n\t"
      "v_mfma_f32_16x16x32_bf16 v[20:23], v[208:211], v[40:43], v[20:23]\n\t"
      "ds_read_b128 v[40:43], v98 offset:12288\n\t"
      "s_cbranch_vccnz L_m6_%=\n\t"
      "v_mfma_f32_16x16x32_bf16 v[24:27], v[152:155], v[44:47], v[24:27]\n\t"
      "v_mfma_f32_16x16x32_bf16 v[28:31], v[208:211], v[44:47], v[28:31]\n\t"
      "ds_read_b128 v[44:47], v98 offset:14336\n\t"
      "L_m6_%=:\n\t"
      "v_cvt_pk_bf16_f32 v84, v72, v73\n\t"
      "v_cvt_pk_bf16_f32 v85, v74, v75\n\t"
      "v_lshlrev_b32 v72, 16, v84\n\t"
      "v_and_b32 v73, 0xffff0000, v84\n\t"
      "v_lshlrev_b32 v74, 16, v85\n\t"
      "v_and_b32 v75, 0xffff0000, v85\n\t"
      "v_rcp_f32 v72, v72\n\t"
      "v_rcp_f32 v73, v73\n\t"
      "v_rcp_f32 v74, v74\n\t"
      "v_rcp_f32 v75, v75\n\t"
      "s_waitcnt lgkmcnt(4)\n\t"
      "v_mfma_f32_16x16x32_bf16 v[0:3], v[156:159], v[48:51], v[0:3]\n\t"
      "v_mfma_f32_16x16x32_bf16 v[4:7], v[212:215], v[48:51], v[4:7]\n\t"
      "ds_read_b128 v[48:51], v99 offset:8192\n\t"
      "v_mfma_f32_16x16x32_bf16 v[8:11], v[156:159], v[52:55], v[8:11]\n\t"
      "v_mfma_f32_16x16x32_bf16 v[12:15], v[212:215], v[52:55], v[12:15]\n\t"
      "ds_read_b128 v[52:55], v99 offset:10240\n\t"
      "v_mfma_f32_16x16x32_bf16 v[16:19], v[156:159], v[56:59], v[16:19]\n\t"
      "v_mfma_f32_16x16x32_bf16 v[20:23], v[212:215], v[56:59], v[20:23]\n\t"
      "ds_read_b128 v[56:59], v99 offset:12288\n\t"
      "s_cbranch_vccnz L_m7_%=\n\t"
      "v_mfma_f32_16x16x32_bf16 v[24:27], v[156:159], v[60:63], v[24:27]\n\t"
      "v_mfma_f32_16x16x32_bf16 v[28:31], v[212:215], v[60:63], v[28:31]\n\t"
      "ds_read_b128 v[60:63], v99 offset:14336\n\t"
      "L_m7_%=:\n\t"
      "s_lshl_b32 s54, s40, 7\n\t"
      "s_add_u32 s54, s54, 160256\n\t"
      "v_lshlrev_b32 v84, 5, v106\n\t"
      "v_add_u32 v84, s54, v84\n\t"
      "ds_write_b128 v84, v[64:67]\n\t"
      "ds_write_b128 v84, v[72:75] offset:16\n\t"
      "s_waitcnt lgkmcnt(3)\n\t"
      "v_readfirstlane_b32 s74, v92\n\t"
      "s_mov_b32 s14, 0\n\t"
      "s_cmp_eq_u32 s74, 4\n\t"
      "s_cbranch_scc1 L_spin11x_%=\n\t"
      "L_spin11_%=:\n\t"
      "ds_read_b32 v92, v127 offset:56\n\t"
      "s_waitcnt lgkmcnt(0)\n\t"
      "s_add_u32 s14, s14, 1\n\t"
      "v_readfirstlane_b32 s74, v92\n\t"
      "s_cmp_lt_u32 s14, 0x100000\n\t"
      "s_cbranch_scc0 L_spin11x_%=\n\t"
      "s_cmp_eq_u32 s74, 4\n\t"
      "s_cbranch_scc0 L_spin11_%=\n\t"
      "L_spin11x_%=:\n\t"
      "ds_read_b32 v92, v127 offset:60\n\t"
      "v_mfma_f32_16x16x32_bf16 v[0:3], v[160:163], v[32:35], v[0:3]\n\t"
      "v_mfma_f32_16x16x32_bf16 v[4:7], v[216:219], v[32:35], v[4:7]\n\t"
      "ds_read_b128 v[32:35], v98 offset:22528\n\t"
      "v_mfma_f32_16x16x32_bf16 v[8:11], v[160:163], v[36:39], v[8:11]\n\t"
      "v_mfma_f32_16x16x32_bf16 v[12:15], v[216:219], v[36:39], v[12:15]\n\t"
      "ds_read_b128 v[36:39], v98 offset:24576\n\t"
      "v_mfma_f32_16x16x32_bf16 v[16:19], v[160:163], v[40:43], v[16:19]\n\t"
      "v_mfma_f32_16x16x32_bf16 v[20:23], v[216:219], v[40:43], v[20:23]\n\t"
      "ds_read_b128 v[40:43], v98 offset:26624\n\t"
      "s_cbranch_vccnz L_m8_%=\n\t"
      "v_mfma_f32_16x16x32_bf16 v[24:27], v[160:163], v[44:47], v[24:27]\n\t"
      "v_mfma_f32_16x16x32_bf16 v[28:31], v[216:219], v[44:47], v[28:31]\n\t"
      "ds_read_b128 v[44:47], v98 offset:28672\n\t"
      "L_m8_%=:\n\t"
      "s_waitcnt lgkmcnt(4)\n\t"
      "v_mfma_f32_16x16x32_bf16 v[0:3], v[164:167], v[48:51], v[0:3]\n\t"
      "v_mfma_f32_16x16x32_bf16 v[4:7], v[220:223], v[48:51], v[4:7]\n\t"
      "ds_read_b128 v[48:51], v99 offset:22528\n\t"
      "v_mfma_f32_16x16x32_bf16 v[8:11], v[164:167], v[52:55], v[8:11]\n\t"
      "v_mfma_f32_16x16x32_bf16 v[12:15], v[220:223], v[52:55], v[12:15]\n\t"
      "ds_read_b128 v[52:55], v99 offset:24576\n\t"
      "v_mfma_f32_16x16x32_bf16 v[16:19], v[164:167], v[56:59], v[16:19]\n\t"
      "v_mfma_f32_16x16x32_bf16 v[20:23], v[220:223], v[56:59], v[20:23]\n\t"
      "ds_read_b128 v[56:59], v99 offset:26624\n\t"
      "s_cbranch_vccnz L_m9_%=\n\t"
      "v_mfma_f32_16x16x32_bf16 v[24:27], v[164:167], v[60:63], v[24:27]\n\t"
      "v_mfma_f32_16x16x32_bf16 v[28:31], v[220:223], v[60:63], v[28:31]\n\t"
      "ds_read_b128 v[60:63], v99 offset:28672\n\t"
      "L_m9_%=:\n\t"
      "s_mov_b64 exec, 1\n\t"
      "ds_add_u32 v127, v126 offset:32\n\t"
      "s_mov_b64 exec, -1\n\t"
      "s_waitcnt lgkmcnt(3)\n\t"
      "v_readfirstlane_b32 s74, v92\n\t"
      "s_mov_b32 s14, 0\n\t"
      "s_cmp_eq_u32 s74, 4\n\t"
      "s_cbranch_scc1 L_spin12x_%=\n\t"
      "L_spin12_%=:\n\t"
      "ds_read_b32 v92, v127 offset:60\n\t"
      "s_waitcnt lgkmcnt(0)\n\t"
      "s_add_u32 s14, s14, 1\n\t"
      "v_readfirstlane_b32 s74, v92\n\t"
      "s_cmp_lt_u32 s14, 0x100000\n\t"
      "s_cbranch_scc0 L_spin12x_%=\n\t"
      "s_cmp_eq_u32 s74, 4\n\t"
      "s_cbranch_scc0 L_spin12_%=\n\t"
      "L_spin12x_%=:\n\t"
      "v_mfma_f32_16x16x32_bf16 v[0:3], v[168:171], v[32:35], v[0:3]\n\t"
      "v_mfma_f32_16x16x32_bf16 v[4:7], v[224:227], v[32:35], v[4:7]\n\t"
      "ds_read_b128 v[32:35], v98 offset:36864\n\t"
      "v_mfma_f32_16x16x32_bf16 v[8:11], v[168:171], v[36:39], v[8:11]\n\t"
      "v_mfma_f32_16x16x32_bf16 v[12:15], v[224:227], v[36:39], v[12:15]\n\t"
      "ds_read_b128 v[36:39], v98 offset:38912\n\t"
      "v_mfma_f32_16x16x32_bf16 v[16:19], v[168:171], v[40:43], v[16:19]\n\t"
      "v_mfma_f32_16x16x32_bf16 v[20:23], v[224:227], v[40:43], v[20:23]\n\t"
      "ds_read_b128 v[40:43], v98 offset:40960\n\t"
      "s_cbranch_vccnz L_m10_%=\n\t"
      "v_mfma_f32_16x16x32_bf16 v[24:27], v[168:171], v[44:47], v[24:27]\n\t"
      "v_mfma_f32_16x16x32_bf16 v[28:31], v[224:227], v[44:47], v[28:31]\n\t"
      "ds_read_b128 v[44:47], v98 offset:43008\n\t"
      "L_m10_%=:\n\t"
      "s_waitcnt lgkmcnt(3)\n\t"
      "v_mfma_f32_16x16x32_bf16 v[0:3], v[172:175], v[48:51], v[0:3]\n\t"
      "v_mfma_f32_16x16x32_bf16 v[4:7], v[228:231], v[48:51], v[4:7]\n\t"
      "ds_read_b128 v[48:51], v99 offset:36864\n\t"
      "v_mfma_f32_16x16x32_bf16 v[8:11], v[172:175], v[52:55], v[8:11]\n\t"
      "v_mfma_f32_16x16x32_bf16 v[12:15], v[228:231], v[52:55], v[12:15]\n\t"
      "ds_read_b128 v[52:55], v99 offset:38912\n\t"
      "v_mfma_f32_16x16x32_bf16 v[16:19], v[172:175], v[56:59], v[16:19]\n\t"
      "v_mfma_f32_16x16x32_bf16 v[20:23], v[228:231], v[56:59], v[20:23]\n\t"
      "ds_read_b128 v[56:59], v99 offset:40960\n\t"
      "s_cbranch_vccnz L_m11_%=\n\t"
      "v_mfma_f32_16x16x32_bf16 v[24:27], v[172:175], v[60:63], v[24:27]\n\t"
      "v_mfma_f32_16x16x32_bf16 v[28:31], v[228:231], v[60:63], v[28:31]\n\t"
      "ds_read_b128 v[60:63], v99 offset:43008\n\t"
      "L_m11_%=:\n\t"
      "s_waitcnt lgkmcnt(3)\n\t"
      "v_mfma_f32_16x16x32_bf16 v[0:3], v[176:179], v[32:35], v[0:3]\n\t"
      "v_mfma_f32_16x16x32_bf16 v[4:7], v[232:235], v[32:35], v[4:7]\n\t"
      "v_mfma_f32_16x16x32_bf16 v[8:11], v[176:179], v[36:39], v[8:11]\n\t"
      "v_mfma_f32_16x16x32_bf16 v[12:15], v[232:235], v[36:39], v[12:15]\n\t"
      "v_mfma_f32_16x16x32_bf16 v[16:19], v[176:179], v[40:43], v[16:19]\n\t"
      "v_mfma_f32_16x16x32_bf16 v[20:23], v[232:235], v[40:43], v[20:23]\n\t"
      "s_cbranch_vccnz L_m12_%=\n\t"
      "v_mfma_f32_16x16x32_bf16 v[24:27], v[176:179], v[44:47], v[24:27]\n\t"
      "v_mfma_f32_16x16x32_bf16 v[28:31], v[232:235], v[44:47], v[28:31]\n\t"
      "L_m12_%=:\n\t"
      "s_waitcnt lgkmcnt(0)\n\t"
      "v_mfma_f32_16x16x32_bf16 v[0:3], v[180:183], v[48:51], v[0:3]\n\t"
      "v_mfma_f32_16x16x32_bf16 v[4:7], v[236:239], v[48:51], v[4:7]\n\t"
      "v_mfma_f32_16x16x32_bf16 v[8:11], v[180:183], v[52:55], v[8:11]\n\t"
      "v_mfma_f32_16x16x32_bf16 v[12:15], v[236:239], v[52:55], v[12:15]\n\t"
      "v_mfma_f32_16x16x32_bf16 v[16:19], v[180:183], v[56:59], v[16:19]\n\t"
      "v_mfma_f32_16x16x32_bf16 v[20:23], v[236:239], v[56:59], v[20:23]\n\t"
      "s_cbranch_vccnz L_m13_%=\n\t"
      "v_mfma_f32_16x16x32_bf16 v[24:27], v[180:183], v[60:63], v[24:27]\n\t"
      "v_mfma_f32_16x16x32_bf16 v[28:31], v[236:239], v[60:63], v[28:31]\n\t"
      "L_m13_%=:\n\t"
      "s_mov_b64 exec, 1\n\t"
      "ds_add_u32 v127, v126 offset:68\n\t"
      "s_mov_b64 exec, -1\n\t"
      "ds_read_b32 v92, v127 offset:32\n\t"
      "s_waitcnt lgkmcnt(0)\n\t"
      "v_readfirstlane_b32 s74, v92\n\t"
      "s_mov_b32 s14, 0\n\t"
      "s_cmp_eq_u32 s74, 4\n\t"
      "s_cbranch_scc1 L_spin13x_%=\n\t"
      "L_spin13_%=:\n\t"
      "ds_read_b32 v92, v127 offset:32\n\t"
      "s_waitcnt lgkmcnt(0)\n\t"
      "s_add_u32 s14, s14, 1\n\t"
      "v_readfirstlane_b32 s74, v92\n\t"
      "s_cmp_lt_u32 s14, 0x100000\n\t"
      "s_cbranch_scc0 L_spin13x_%=\n\t"
      "s_cmp_eq_u32 s74, 4\n\t"
      "s_cbranch_scc0 L_spin13_%=\n\t"
      "L_spin13x_%=:\n\t"
      "ds_read_b32 v92, v127 offset:64\n\t"
      "s_waitcnt lgkmcnt(0)\n\t"
      "v_readfirstlane_b32 s74, v92\n\t"
      "s_mov_b32 s14, 0\n\t"
      "s_cmp_eq_u32 s74, 4\n\t"
      "s_cbranch_scc1 L_spin14x_%=\n\t"
      "L_spin14_%=:\n\t"
      "ds_read_b32 v92, v127 offset:64\n\t"
      "s_waitcnt lgkmcnt(0)\n\t"
      "s_add_u32 s14, s14, 1\n\t"
      "v_readfirstlane_b32 s74, v92\n\t"
      "s_cmp_lt_u32 s14, 0x100000\n\t"
      "s_cbranch_scc0 L_spin14x_%=\n\t"
      "s_cmp_eq_u32 s74, 4\n\t"
      "s_cbranch_scc0 L_spin14_%=\n\t"
      "L_spin14x_%=:\n\t"
      "s_lshl_b32 s54, s59, 8\n\t"
      "s_add_u32 s54, s54, 160256\n\t"
      "v_lshlrev_b32 v116, 5, v106\n\t"
      "v_add_u32 v116, s54, v116\n\t"
      "ds_read_b128 v[64:67], v116 offset:0\n\t"
      "ds_read_b128 v[72:75], v116 offset:16\n\t"
      "ds_read_b128 v[68:71], v116 offset:128\n\t"
      "ds_read_b128 v[76:79], v116 offset:144\n\t"
      "s_lshl_b32 s54, s60, 8\n\t"
      "s_add_u32 s54, s54, 159744\n\t"
      "v_lshlrev_b32 v117, 2, v105\n\t"
      "v_add_u32 v117, s54, v117\n\t"
      "ds_read_b32 v80, v117 offset:0\n\t"
      "ds_read_b32 v81, v117 offset:64\n\t"
      "ds_read_b32 v82, v117 offset:128\n\t"
      "ds_read_b32 v83, v117 offset:192\n\t"
      "s_waitcnt lgkmcnt(0)\n\t"
      "v_pk_fma_f32 v[0:1], v[64:65], v[80:81], v[0:1] op_sel_hi:[1,0,1] neg_lo:[1,0,0] neg_hi:[1,0,0]\n\t"
      "v_pk_fma_f32 v[2:3], v[66:67], v[80:81], v[2:3] op_sel_hi:[1,0,1] neg_lo:[1,0,0] neg_hi:[1,0,0]\n\t"
      "v_pk_mul_f32 v[0:1], v[72:73], v[0:1]\n\t"
      "v_pk_mul_f32 v[2:3], v[74:75], v[2:3]\n\t"
      "v_cvt_pk_bf16_f32 v84, v0, v1\n\t"
      "v_cvt_pk_bf16_f32 v85, v2, v3\n\t"
      "v_pk_fma_f32 v[4:5], v[68:69], v[80:81], v[4:5] op_sel_hi:[1,0,1] neg_lo:[1,0,0] neg_hi:[1,0,0]\n\t"
      "v_pk_fma_f32 v[6:7], v[70:71], v[80:81], v[6:7] op_sel_hi:[1,0,1] neg_lo:[1,0,0] neg_hi:[1,0,0]\n\t"
      "v_pk_mul_f32 v[4:5], v[76:77], v[4:5]\n\t"
      "v_pk_mul_f32 v[6:7], v[78:79], v[6:7]\n\t"
      "v_cvt_pk_bf16_f32 v86, v4, v5\n\t"
      "v_cvt_pk_bf16_f32 v87, v6, v7\n\t"
      "buffer_store_dwordx4 v[84:87], v107, s[64:67], s75 offen offset:0 sc1\n\t"
      "v_pk_fma_f32 v[8:9], v[64:65], v[80:81], v[8:9] op_sel:[0,1,0] op_sel_hi:[1,1,1] neg_lo:[1,0,0] neg_hi:[1,0,0]\n\t"
      "v_pk_fma_f32 v[10:11], v[66:67], v[80:81], v[10:11] op_sel:[0,1,0] op_sel_hi:[1,1,1] neg_lo:[1,0,0] neg_hi:[1,0,0]\n\t"
      "v_pk_mul_f32 v[8:9], v[72:73], v[8:9]\n\t"
      "v_pk_mul_f32 v[10:11], v[74:75], v[10:11]\n\t"
      "v_cvt_pk_bf16_f32 v88, v8, v9\n\t"
      "v_cvt_pk_bf16_f32 v89, v10, v11\n\t"
      "v_pk_fma_f32 v[12:13], v[68:69], v[80:81], v[12:13] op_sel:[0,1,0] op_sel_hi:[1,1,1] neg_lo:[1,0,0] neg_hi:[1,0,0]\n\t"
      "v_pk_fma_f32 v[14:15], v[70:71], v[80:81], v[14:15] op_sel:[0,1,0] op_sel_hi:[1,1,1] neg_lo:[1,0,0] neg_hi:[1,0,0]\n\t"
      "v_pk_mul_f32 v[12:13], v[76:77], v[12:13]\n\t"
      "v_pk_mul_f32 v[14:15], v[78:79], v[14:15]\n\t"
      "v_cvt_pk_bf16_f32 v90, v12, v13\n\t"
      "v_cvt_pk_bf16_f32 v91, v14, v15\n\t"
      "buffer_store_dwordx4 v[88:91], v107, s[64:67], s75 offen offset:1024 sc1\n\t"
      "v_pk_fma_f32 v[16:17], v[64:65], v[82:83], v[16:17] op_sel_hi:[1,0,1] neg_lo:[1,0,0] neg_hi:[1,0,0]\n\t"
      "v_pk_fma_f32 v[18:19], v[66:67], v[82:83], v[18:19] op_sel_hi:[1,0,1] neg_lo:[1,0,0] neg_hi:[1,0,0]\n\t"
      "v_pk_mul_f32 v[16:17], v[72:73], v[16:17]\n\t"
      "v_pk_mul_f32 v[18:19], v[74:75], v[18:19]\n\t"
      "v_cvt_pk_bf16_f32 v84, v16, v17\n\t"
      "v_cvt_pk_bf16_f32 v85, v18, v19\n\t"
      "v_pk_fma_f32 v[20:21], v[68:69], v[82:83], v[20:21] op_sel_hi:[1,0,1] neg_lo:[1,0,0] neg_hi:[1,0,0]\n\t"
      "v_pk_fma_f32 v[22:23], v[70:71], v[82:83], v[22:23] op_sel_hi:[1,0,1] neg_lo:[1,0,0] neg_hi:[1,0,0]\n\t"
      "v_pk_mul_f32 v[20:21], v[76:77], v[20:21]\n\t"
      "v_pk_mul_f32 v[22:23], v[78:79], v[22:23]\n\t"
      "v_cvt_pk_bf16_f32 v86, v20, v21\n\t"
      "v_cvt_pk_bf16_f32 v87, v22, v23\n\t"
      "buffer_store_dwordx4 v[84:87], v107, s[64:67], s75 offen offset:2048 sc1\n\t"
      "s_cbranch_vccnz L_epi3_%=\n\t"
      "v_pk_fma_f32 v[24:25], v[64:65], v[82:83], v[24:25] op_sel:[0,1,0] op_sel_hi:[1,1,1] neg_lo:[1,0,0] neg_hi:[1,0,0]\n\t"
      "v_pk_fma_f32 v[26:27], v[66:67], v[82:83], v[26:27] op_sel:[0,1,0] op_sel_hi:[1,1,1] neg_lo:[1,0,0] neg_hi:[1,0,0]\n\t"
      "v_pk_mul_f32 v[24:25], v[72:73], v[24:25]\n\t"
      "v_pk_mul_f32 v[26:27], v[74:75], v[26:27]\n\t"
      "v_cvt_pk_bf16_f32 v88, v24, v25\n\t"
      "v_cvt_pk_bf16_f32 v89, v26, v27\n\t"
      "v_pk_fma_f32 v[28:29], v[68:69], v[82:83], v[28:29] op_sel:[0,1,0] op_sel_hi:[1,1,1] neg_lo:[1,0,0] neg_hi:[1,0,0]\n\t"
      "v_pk_fma_f32 v[30:31], v[70:71], v[82:83], v[30:31] op_sel:[0,1,0] op_sel_hi:[1,1,1] neg_lo:[1,0,0] neg_hi:[1,0,0]\n\t"
      "v_pk_mul_f32 v[28:29], v[76:77], v[28:29]\n\t"
      "v_pk_mul_f32 v[30:31], v[78:79], v[30:31]\n\t"
      "v_cvt_pk_bf16_f32 v90, v28, v29\n\t"
      "v_cvt_pk_bf16_f32 v91, v30, v31\n\t"
      "buffer_store_dwordx4 v[88:91], v107, s[64:67], s75 offen offset:3072 sc1\n\t"
      "L_epi3_%=:\n\t"
      "s_mov_b64 exec, 1\n\t"
      "ds_add_u32 v127, v126 offset:72\n\t"
      "s_mov_b64 exec, -1\n\t"
      "s_waitcnt vmcnt(0)\n\t"
      "v_mov_b32 v116, s44\n\t"
      "v_mov_b32 v117, s45\n\t"
      "s_lshl_b32 s54, s43, 7\n\t"
      "s_lshl_b32 s55, s40, 3\n\t"
      "s_add_u32 s54, s54, s55\n\t"
      "s_add_u32 s54, s54, 4096\n\t"
      "v_mov_b32 v118, 0\n\t"
      "s_mov_b64 vcc, exec\n\t"
      "s_mov_b64 exec, 1\n\t"
      "buffer_store_dwordx2 v[116:117], v118, s[64:67], s54 offen sc1\n\t"
      "s_mov_b64 exec, vcc\n\t"
      "ds_read_b32 v92, v127 offset:68\n\t"
      "s_waitcnt lgkmcnt(0)\n\t"
      "v_readfirstlane_b32 s74, v92\n\t"
      "s_mov_b32 s14, 0\n\t"
      "s_cmp_eq_u32 s74, 4\n\t"
      "s_cbranch_scc1 L_spin15x_%=\n\t"
      "L_spin15_%=:\n\t"
      "ds_read_b32 v92, v127 offset:68\n\t"
      "s_waitcnt lgkmcnt(0)\n\t"
      "s_add_u32 s14, s14, 1\n\t"
      "v_readfirstlane_b32 s74, v92\n\t"
      "s_cmp_lt_u32 s14, 0x100000\n\t"
      "s_cbranch_scc0 L_spin15x_%=\n\t"
      "s_cmp_eq_u32 s74, 4\n\t"
      "s_cbranch_scc0 L_spin15_%=\n\t"
      "L_spin15x_%=:\n\t"
      "s_add_u32 s15, s18, 0\n\t"
      "s_and_b32 s15, s15, 15\n\t"
      "s_lshl_b32 s15, s15, 6\n\t"
      "s_add_u32 s15, s15, s12\n\t"
      "s_add_u32 m0, s58, 0\n\t"
      "s_nop 0\n\t"
      "buffer_load_dwordx4 v110, s[36:39], s15 offen lds\n\t"
      "s_add_u32 m0, s58, 1024\n\t"
      "s_nop 0\n\t"
      "buffer_load_dwordx4 v111, s[36:39], s15 offen lds\n\t"
      "s_add_u32 s15, s18, 0\n\t"
      "s_and_b32 s15, s15, 15\n\t"
      "s_lshl_b32 s15, s15, 6\n\t"
      "s_add_u32 s15, s15, s13\n\t"
      "s_add_u32 m0, s58, 2048\n\t"
      "s_nop 0\n\t"
      "buffer_load_dwordx4 v110, s[36:39], s15 offen lds\n\t"
      "s_add_u32 m0, s58, 3072\n\t"
      "s_nop 0\n\t"
      "buffer_load_dwordx4 v111, s[36:39], s15 offen lds\n\t"
      "s_add_u32 s15, s18, 2\n\t"
      "s_and_b32 s15, s15, 15\n\t"
      "s_lshl_b32 s15, s15, 6\n\t"
      "s_add_u32 s15, s15, s12\n\t"
      "s_add_u32 m0, s58, 4096\n\t"
      "s_nop 0\n\t"
      "buffer_load_dwordx4 v110, s[36:39], s15 offen lds\n\t"
      "s_add_u32 m0, s58, 5120\n\t"
      "s_nop 0\n\t"
      "buffer_load_dwordx4 v111, s[36:39], s15 offen lds\n\t"
      "s_add_u32 s15, s18, 2\n\t"
      "s_and_b32 s15, s15, 15\n\t"
      "s_lshl_b32 s15, s15, 6\n\t"
      "s_add_u32 s15, s15, s13\n\t"
      "s_add_u32 m0, s58, 6144\n\t"
      "s_nop 0\n\t"
      "buffer_load_dwordx4 v110, s[36:39], s15 offen lds\n\t"
      "s_add_u32 m0, s58, 7168\n\t"
      "s_nop 0\n\t"
      "buffer_load_dwordx4 v111, s[36:39], s15 offen lds\n\t"
      "s_add_u32 s15, s18, 4\n\t"
      "s_and_b32 s15, s15, 15\n\t"
      "s_lshl_b32 s15, s15, 6\n\t"
      "s_add_u32 s15, s15, s12\n\t"
      "s_add_u32 m0, s58, 8192\n\t"
      "s_nop 0\n\t"
      "buffer_load_dwordx4 v110, s[36:39], s15 offen lds\n\t"
      "s_add_u32 m0, s58, 9216\n\t"
      "s_nop 0\n\t"
      "buffer_load_dwordx4 v111, s[36:39], s15 offen lds\n\t"
      "s_add_u32 s15, s18, 4\n\t"
      "s_and_b32 s15, s15, 15\n\t"
      "s_lshl_b32 s15, s15, 6\n\t"
      "s_add_u32 s15, s15, s13\n\t"
      "s_add_u32 m0, s58, 10240\n\t"
      "s_nop 0\n\t"
      "buffer_load_dwordx4 v110, s[36:39], s15 offen lds\n\t"
      "s_add_u32 m0, s58, 11264\n\t"
      "s_nop 0\n\t"
      "buffer_load_dwordx4 v111, s[36:39], s15 offen lds\n\t"
      "s_add_u32 s15, s18, 6\n\t"
      "s_and_b32 s15, s15, 15\n\t"
      "s_lshl_b32 s15, s15, 6\n\t"
      "s_add_u32 s15, s15, s12\n\t"
      "s_add_u32 m0, s58, 12288\n\t"
      "s_nop 0\n\t"
      "buffer_load_dwordx4 v110, s[36:39], s15 offen lds\n\t"
      "s_add_u32 m0, s58, 13312\n\t"
      "s_nop 0\n\t"
      "buffer_load_dwordx4 v111, s[36:39], s15 offen lds\n\t"
      "s_add_u32 s15, s18, 6\n\t"
      "s_and_b32 s15, s15, 15\n\t"
      "s_lshl_b32 s15, s15, 6\n\t"
      "s_add_u32 s15, s15, s13\n\t"
      "s_add_u32 m0, s58, 14336\n\t"
      "s_nop 0\n\t"
      "buffer_load_dwordx4 v110, s[36:39], s15 offen lds\n\t"
      "s_add_u32 m0, s58, 15360\n\t"
      "s_nop 0\n\t"
      "buffer_load_dwordx4 v111, s[36:39], s15 offen lds\n\t"
      "s_add_u32 s15, s18, 8\n\t"
      "s_and_b32 s15, s15, 15\n\t"
      "s_lshl_b32 s15, s15, 6\n\t"
      "s_add_u32 s15, s15, s12\n\t"
      "s_add_u32 m0, s58, 16384\n\t"
      "s_nop 0\n\t"
      "buffer_load_dwordx4 v110, s[36:39], s15 offen lds\n\t"
      "s_add_u32 m0, s58, 17408\n\t"
      "s_nop 0\n\t"
      "buffer_load_dwordx4 v111, s[36:39], s15 offen lds\n\t"
      "s_add_u32 s15, s18, 8\n\t"
      "s_and_b32 s15, s15, 15\n\t"
      "s_lshl_b32 s15, s15, 6\n\t"
      "s_add_u32 s15, s15, s13\n\t"
      "s_add_u32 m0, s58, 18432\n\t"
      "s_nop 0\n\t"
      "buffer_load_dwordx4 v110, s[36:39], s15 offen lds\n\t"
      "s_add_u32 m0, s58, 19456\n\t"
      "s_nop 0\n\t"
      "buffer_load_dwordx4 v111, s[36:39], s15 offen lds\n\t"
      "s_waitcnt vmcnt(16)\n\t"
      "ds_read_b128 v[128:131], v122 offset:0\n\t"
      "ds_read_b128 v[136:139], v123 offset:0\n\t"
      "ds_read_b128 v[132:135], v122 offset:2048\n\t"
      "ds_read_b128 v[140:143], v123 offset:2048\n\t"
      "s_waitcnt lgkmcnt(0)\n\t"
      "s_add_u32 s15, s18, 10\n\t"
      "s_and_b32 s15, s15, 15\n\t"
      "s_lshl_b32 s15, s15, 6\n\t"
      "s_add_u32 s15, s15, s12\n\t"
      "s_add_u32 m0, s58, 0\n\t"
      "s_nop 0\n\t"
      "buffer_load_dwordx4 v110, s[36:39], s15 offen lds\n\t"
      "s_add_u32 m0, s58, 1024\n\t"
      "s_nop 0\n\t"
      "buffer_load_dwordx4 v111, s[36:39], s15 offen lds\n\t"
      "s_add_u32 s15, s18, 10\n\t"
      "s_and_b32 s15, s15, 15\n\t"
      "s_lshl_b32 s15, s15, 6\n\t"
      "s_add_u32 s15, s15, s13\n\t"
      "s_add_u32 m0, s58, 2048\n\t"
      "s_nop 0\n\t"
      "buffer_load_dwordx4 v110, s[36:39], s15 offen lds\n\t"
      "s_add_u32 m0, s58, 3072\n\t"
      "s_nop 0\n\t"
      "buffer_load_dwordx4 v111, s[36:39], s15 offen lds\n\t"
      "s_waitcnt vmcnt(16)\n\t"
      "ds_read_b128 v[144:147], v122 offset:4096\n\t"
      "ds_read_b128 v[152:155], v123 offset:4096\n\t"
      "ds_read_b128 v[148:151], v122 offset:6144\n\t"
      "ds_read_b128 v[156:159], v123 offset:6144\n\t"
      "s_waitcnt lgkmcnt(0)\n\t"
      "s_add_u32 s15, s18, 12\n\t"
      "s_and_b32 s15, s15, 15\n\t"
      "s_lshl_b32 s15, s15, 6\n\t"
      "s_add_u32 s15, s15, s12\n\t"
      "s_add_u32 m0, s58, 4096\n\t"
      "s_nop 0\n\t"
      "buffer_load_dwordx4 v110, s[36:39], s15 offen lds\n\t"
      "s_add_u32 m0, s58, 5120\n\t"
      "s_nop 0\n\t"
      "buffer_load_dwordx4 v111, s[36:39], s15 offen lds\n\t"
      "s_add_u32 s15, s18, 12\n\t"
      "s_and_b32 s15, s15, 15\n\t"
      "s_lshl_b32 s15, s15, 6\n\t"
      "s_add_u32 s15, s15, s13\n\t"
      "s_add_u32 m0, s58, 6144\n\t"
      "s_nop 0\n\t"
      "buffer_load_dwordx4 v110, s[36:39], s15 offen lds\n\t"
      "s_add_u32 m0, s58, 7168\n\t"
      "s_nop 0\n\t"
      "buffer_load_dwordx4 v111, s[36:39], s15 offen lds\n\t"
      "s_waitcnt vmcnt(16)\n\t"
      "ds_read_b128 v[160:163], v122 offset:8192\n\t"
      "ds_read_b128 v[168:171], v123 offset:8192\n\t"
      "ds_read_b128 v[164:167], v122 offset:10240\n\t"
      "ds_read_b128 v[172:175], v123 offset:10240\n\t"
      "s_waitcnt lgkmcnt(0)\n\t"
      "s_add_u32 s15, s18, 14\n\t"
      "s_and_b32 s15, s15, 15\n\t"
      "s_lshl_b32 s15, s15, 6\n\t"
      "s_add_u32 s15, s15, s12\n\t"
      "s_add_u32 m0, s58, 8192\n\t"
      "s_nop 0\n\t"
      "buffer_load_dwordx4 v110, s[36:39], s15 offen lds\n\t"
      "s_add_u32 m0, s58, 9216\n\t"
      "s_nop 0\n\t"
      "buffer_load_dwordx4 v111, s[36:39], s15 offen lds\n\t"
      "s_add_u32 s15, s18, 14\n\t"
      "s_and_b32 s15, s15, 15\n\t"
      "s_lshl_b32 s15, s15, 6\n\t"
      "s_add_u32 s15, s15, s13\n\t"
      "s_add_u32 m0, s58, 10240\n\t"
      "s_nop 0\n\t"
      "buffer_load_dwordx4 v110, s[36:39], s15 offen lds\n\t"
      "s_add_u32 m0, s58, 11264\n\t"
      "s_nop 0\n\t"
      "buffer_load_dwordx4 v111, s[36:39], s15 offen lds\n\t"
      "s_waitcnt vmcnt(16)\n\t"
      "ds_read_b128 v[176:179], v122 offset:12288\n\t"
      "ds_read_b128 v[184:187], v123 offset:12288\n\t"
      "ds_read_b128 v[180:183], v122 offset:14336\n\t"
      "ds_read_b128 v[188:191], v123 offset:14336\n\t"
      "s_waitcnt vmcnt(12)\n\t"
      "ds_read_b128 v[192:195], v122 offset:16384\n\t"
      "ds_read_b128 v[200:203], v123 offset:16384\n\t"
      "ds_read_b128 v[196:199], v122 offset:18432\n\t"
      "ds_read_b128 v[204:207], v123 offset:18432\n\t"
      "s_waitcnt vmcnt(8)\n\t"
      "ds_read_b128 v[208:211], v122 offset:0\n\t"
      "ds_read_b128 v[216:219], v123 offset:0\n\t"
      "ds_read_b128 v[212:215], v122 offset:2048\n\t"
      "ds_read_b128 v[220:223], v123 offset:2048\n\t"
      "s_waitcnt vmcnt(4)\n\t"
      "ds_read_b128 v[224:227], v122 offset:4096\n\t"
      "ds_read_b128 v[232:235], v123 offset:4096\n\t"
      "ds_read_b128 v[228:231], v122 offset:6144\n\t"
      "ds_read_b128 v[236:239], v123 offset:6144\n\t"
      "s_waitcnt vmcnt(0)\n\t"
      "ds_read_b128 v[240:243], v122 offset:8192\n\t"
      "ds_read_b128 v[248:251], v123 offset:8192\n\t"
      "ds_read_b128 v[244:247], v122 offset:10240\n\t"
      "ds_read_b128 v[252:255], v123 offset:10240\n\t"
      "L_p1join_%=:\n\t"
      "s_waitcnt lgkmcnt(0)\n\t"
      "s_barrier\n\t"
      "s_add_u32 s15, s18, 0\n\t"
      "s_and_b32 s15, s15, 15\n\t"
      "s_mul_i32 s15, s15, 28672\n\t"
      "s_add_u32 m0, s34, 0\n\t"
      "s_add_u32 s15, s15, s61\n\t"
      "buffer_load_dwordx4 v107, s[64:67], s15 offen  lds\n\t"
      "s_add_u32 s15, s18, 1\n\t"
      "s_and_b32 s15, s15, 15\n\t"
      "s_mul_i32 s15, s15, 28672\n\t"
      "s_add_u32 m0, s34, 4096\n\t"
      "s_add_u32 s15, s15, s61\n\t"
      "buffer_load_dwordx4 v107, s[64:67], s15 offen  lds\n\t"
      "s_add_u32 s15, s18, 2\n\t"
      "s_and_b32 s15, s15, 15\n\t"
      "s_mul_i32 s15, s15, 28672\n\t"
      "s_add_u32 m0, s34, 16384\n\t"
      "s_add_u32 s15, s15, s61\n\t"
      "buffer_load_dwordx4 v107, s[64:67], s15 offen  lds\n\t"
      "s_add_u32 s15, s18, 3\n\t"
      "s_and_b32 s15, s15, 15\n\t"
      "s_mul_i32 s15, s15, 28672\n\t"
      "s_add_u32 m0, s34, 20480\n\t"
      "s_add_u32 s15, s15, s61\n\t"
      "buffer_load_dwordx4 v107, s[64:67], s15 offen  lds\n\t"
      "s_add_u32 s15, s18, 4\n\t"
      "s_and_b32 s15, s15, 15\n\t"
      "s_mul_i32 s15, s15, 28672\n\t"
      "s_add_u32 m0, s34, 32768\n\t"
      "s_add_u32 s15, s15, s61\n\t"
      "buffer_load_dwordx4 v107, s[64:67], s15 offen  lds\n\t"
      "s_add_u32 s15, s18, 5\n\t"
      "s_and_b32 s15, s15, 15\n\t"
      "s_mul_i32 s15, s15, 28672\n\t"
      "s_add_u32 m0, s34, 36864\n\t"
      "s_add_u32 s15, s15, s61\n\t"
      "buffer_load_dwordx4 v107, s[64:67], s15 offen  lds\n\t"
      "s_waitcnt vmcnt(4)\n\t"
      "s_barrier\n\t"
      "ds_read_b128 v[56:59], v108 offset:0\n\t"
      "ds_read_b128 v[60:63], v108 offset:1024\n\t"
      "ds_read_b128 v[64:67], v108 offset:2048\n\t"
      "ds_read_b128 v[68:71], v108 offset:3072\n\t"
      "ds_read_b128 v[72:75], v108 offset:4096\n\t"
      "ds_read_b128 v[76:79], v108 offset:5120\n\t"
      "ds_read_b128 v[80:83], v108 offset:6144\n\t"
      "ds_read_b128 v[84:87], v108 offset:7168\n\t"
      "s_waitcnt vmcnt(2)\n\t"
      "s_barrier\n\t"
      "s_waitcnt lgkmcnt(7)\n\t"
      "v_mfma_f32_16x16x32_bf16 v[0:3], v[56:59], v[128:131], 0\n\t"
      "v_mfma_f32_16x16x32_bf16 v[4:7], v[56:59], v[132:135], 0\n\t"
      "ds_read_b128 v[56:59], v108 offset:16384\n\t"
      "s_add_u32 s15, s18, 6\n\t"
      "s_and_b32 s15, s15, 15\n\t"
      "s_mul_i32 s15, s15, 28672\n\t"
      "s_add_u32 m0, s34, 49152\n\t"
      "s_add_u32 s15, s15, s61\n\t"
      "buffer_load_dwordx4 v107, s[64:67], s15 offen  lds\n\t"
      "s_waitcnt lgkmcnt(7)\n\t"
      "v_mfma_f32_16x16x32_bf16 v[8:11], v[60:63], v[128:131], 0\n\t"
      "v_mfma_f32_16x16x32_bf16 v[12:15], v[60:63], v[132:135], 0\n\t"
      "ds_read_b128 v[60:63], v108 offset:17408\n\t"
      "s_waitcnt lgkmcnt(7)\n\t"
      "v_mfma_f32_16x16x32_bf16 v[16:19], v[64:67], v[128:131], 0\n\t"
      "v_mfma_f32_16x16x32_bf16 v[20:23], v[64:67], v[132:135], 0\n\t"
      "ds_read_b128 v[64:67], v108 offset:18432\n\t"
      "s_add_u32 s15, s18, 7\n\t"
      "s_and_b32 s15, s15, 15\n\t"
      "s_mul_i32 s15, s15, 28672\n\t"
      "s_add_u32 m0, s34, 53248\n\t"
      "s_add_u32 s15, s15, s61\n\t"
      "buffer_load_dwordx4 v107, s[64:67], s15 offen  lds\n\t"
      "s_waitcnt lgkmcnt(7)\n\t"
      "v_mfma_f32_16x16x32_bf16 v[24:27], v[68:71], v[128:131], 0\n\t"
      "v_mfma_f32_16x16x32_bf16 v[28:31], v[68:71], v[132:135], 0\n\t"
      "ds_read_b128 v[68:71], v108 offset:19456\n\t"
      "s_waitcnt lgkmcnt(7)\n\t"
      "v_mfma_f32_16x16x32_bf16 v[0:3], v[72:75], v[136:139], v[0:3]\n\t"
      "v_mfma_f32_16x16x32_bf16 v[4:7], v[72:75], v[140:143], v[4:7]\n\t"
      "ds_read_b128 v[72:75], v108 offset:20480\n\t"
      "s_add_u32 s15, s18, 8\n\t"
      "s_and_b32 s15, s15, 15\n\t"
      "s_mul_i32 s15, s15, 28672\n\t"
      "s_add_u32 m0, s34, 65536\n\t"
      "s_add_u32 s15, s15, s61\n\t"
      "buffer_load_dwordx4 v107, s[64:67], s15 offen  lds\n\t"
      "s_waitcnt lgkmcnt(7)\n\t"
      "v_mfma_f32_16x16x32_bf16 v[8:11], v[76:79], v[136:139], v[8:11]\n\t"
      "v_mfma_f32_16x16x32_bf16 v[12:15], v[76:79], v[140:143], v[12:15]\n\t"
      "ds_read_b128 v[76:79], v108 offset:21504\n\t"
      "v_mov_b32 v88, -1\n\t"
      "v_mov_b32 v89, -1\n\t"
      "v_mov_b32 v90, -1\n\t"
      "v_mov_b32 v91, -1\n\t"
      "s_lshl_b32 s75, s43, 3\n\t"
      "s_add_u32 s75, s75, s40\n\t"
      "s_lshl_b32 s75, s75, 10\n\t"
      "s_add_u32 s75, s75, 5242880\n\t"
      "s_waitcnt lgkmcnt(7)\n\t"
      "v_mfma_f32_16x16x32_bf16 v[16:19], v[80:83], v[136:139], v[16:19]\n\t"
      "v_mfma_f32_16x16x32_bf16 v[20:23], v[80:83], v[140:143], v[20:23]\n\t"
      "ds_read_b128 v[80:83], v108 offset:22528\n\t"
      "s_add_u32 s15, s18, 9\n\t"
      "s_and_b32 s15, s15, 15\n\t"
      "s_mul_i32 s15, s15, 28672\n\t"
      "s_add_u32 m0, s34, 69632\n\t"
      "s_add_u32 s15, s15, s61\n\t"
      "buffer_load_dwordx4 v107, s[64:67], s15 offen  lds\n\t"
      "s_waitcnt lgkmcnt(7)\n\t"
      "v_mfma_f32_16x16x32_bf16 v[24:27], v[84:87], v[136:139], v[24:27]\n\t"
      "v_mfma_f32_16x16x32_bf16 v[28:31], v[84:87], v[140:143], v[28:31]\n\t"
      "ds_read_b128 v[84:87], v108 offset:23552\n\t"
      "buffer_store_dwordx4 v[88:91], v107, s[64:67], s75 offen sc1\n\t"
      "s_add_u32 s75, s75, 2097152\n\t"
      "s_waitcnt vmcnt(5)\n\t"
      "s_barrier\n\t"
      "s_waitcnt lgkmcnt(7)\n\t"
      "v_mfma_f32_16x16x32_bf16 v[0:3], v[56:59], v[144:147], v[0:3]\n\t"
      "v_mfma_f32_16x16x32_bf16 v[4:7], v[56:59], v[148:151], v[4:7]\n\t"
      "ds_read_b128 v[56:59], v108 offset:32768\n\t"
      "s_add_u32 s15, s18, 10\n\t"
      "s_and_b32 s15, s15, 15\n\t"
      "s_mul_i32 s15, s15, 28672\n\t"
      "s_add_u32 m0, s34, 81920\n\t"
      "s_add_u32 s15, s15, s61\n\t"
      "buffer_load_dwordx4 v107, s[64:67], s15 offen  lds\n\t"
      "s_waitcnt lgkmcnt(7)\n\t"
      "v_mfma_f32_16x16x32_bf16 v[8:11], v[60:63], v[144:147], v[8:11]\n\t"
      "v_mfma_f32_16x16x32_bf16 v[12:15], v[60:63], v[148:151], v[12:15]\n\t"
      "ds_read_b128 v[60:63], v108 offset:33792\n\t"
      "buffer_store_dwordx4 v[88:91], v107, s[64:67], s75 offen sc1\n\t"
      "s_add_u32 s75, s75, 2097152\n\t"
      "s_waitcnt lgkmcnt(7)\n\t"
      "v_mfma_f32_16x16x32_bf16 v[16:19], v[64:67], v[144:147], v[16:19]\n\t"
      "v_mfma_f32_16x16x32_bf16 v[20:23], v[64:67], v[148:151], v[20:23]\n\t"
      "ds_read_b128 v[64:67], v108 offset:34816\n\t"
      "s_add_u32 s15, s18, 11\n\t"
      "s_and_b32 s15, s15, 15\n\t"
      "s_mul_i32 s15, s15, 28672\n\t"
      "s_add_u32 m0, s34, 86016\n\t"
      "s_add_u32 s15, s15, s61\n\t"
      "buffer_load_dwordx4 v107, s[64:67], s15 offen  lds\n\t"
      "s_waitcnt lgkmcnt(7)\n\t"
      "v_mfma_f32_16x16x32_bf16 v[24:27], v[68:71], v[144:147], v[24:27]\n\t"
      "v_mfma_f32_16x16x32_bf16 v[28:31], v[68:71], v[148:151], v[28:31]\n\t"
      "ds_read_b128 v[68:71], v108 offset:35840\n\t"
      "s_waitcnt lgkmcnt(7)\n\t"
      "v_mfma_f32_16x16x32_bf16 v[0:3], v[72:75], v[152:155], v[0:3]\n\t"
      "v_mfma_f32_16x16x32_bf16 v[4:7], v[72:75], v[156:159], v[4:7]\n\t"
      "ds_read_b128 v[72:75], v108 offset:36864\n\t"
      "s_add_u32 s15, s18, 12\n\t"
      "s_and_b32 s15, s15, 15\n\t"
      "s_mul_i32 s15, s15, 28672\n\t"
      "s_add_u32 m0, s34, 0\n\t"
      "s_add_u32 s15, s15, s61\n\t"
      "buffer_load_dwordx4 v107, s[64:67], s15 offen  lds\n\t"
      "s_waitcnt lgkmcnt(7)\n\t"
      "v_mfma_f32_16x16x32_bf16 v[8:11], v[76:79], v[152:155], v[8:11]\n\t"
      "v_mfma_f32_16x16x32_bf16 v[12:15], v[76:79], v[156:159], v[12:15]\n\t"
      "ds_read_b128 v[76:79], v108 offset:37888\n\t"
      "buffer_store_dwordx4 v[88:91], v107, s[64:67], s75 offen sc1\n\t"
      "s_add_u32 s75, s75, 2097152\n\t"
      "s_waitcnt lgkmcnt(7)\n\t"
      "v_mfma_f32_16x16x32_bf16 v[16:19], v[80:83], v[152:155], v[16:19]\n\t"
      "v_mfma_f32_16x16x32_bf16 v[20:23], v[80:83], v[156:159], v[20:23]\n\t"
      "ds_read_b128 v[80:83], v108 offset:38912\n\t"
      "s_add_u32 s15, s18, 13\n\t"
      "s_and_b32 s15, s15, 15\n\t"
      "s_mul_i32 s15, s15, 28672\n\t"
      "s_add_u32 m0, s34, 4096\n\t"
      "s_add_u32 s15, s15, s61\n\t"
      "buffer_load_dwordx4 v107, s[64:67], s15 offen  lds\n\t"
      "s_waitcnt lgkmcnt(7)\n\t"
      "v_mfma_f32_16x16x32_bf16 v[24:27], v[84:87], v[152:155], v[24:27]\n\t"
      "v_mfma_f32_16x16x32_bf16 v[28:31], v[84:87], v[156:159], v[28:31]\n\t"
      "ds_read_b128 v[84:87], v108 offset:39936\n\t"
      "s_waitcnt vmcnt(9)\n\t"
      "s_barrier\n\t"
      "s_waitcnt lgkmcnt(7)\n\t"
      "v_mfma_f32_16x16x32_bf16 v[0:3], v[56:59], v[160:163], v[0:3]\n\t"
      "v_mfma_f32_16x16x32_bf16 v[4:7], v[56:59], v[164:167], v[4:7]\n\t"
      "ds_read_b128 v[56:59], v108 offset:49152\n\t"
      "s_add_u32 s15, s18, 14\n\t"
      "s_and_b32 s15, s15, 15\n\t"
      "s_mul_i32 s15, s15, 28672\n\t"
      "s_add_u32 m0, s34, 16384\n\t"
      "s_add_u32 s15, s15, s61\n\t"
      "buffer_load_dwordx4 v107, s[64:67], s15 offen  lds\n\t"
      "s_waitcnt lgkmcnt(7)\n\t"
      "v_mfma_f32_16x16x32_bf16 v[8:11], v[60:63], v[160:163], v[8:11]\n\t"
      "v_mfma_f32_16x16x32_bf16 v[12:15], v[60:63], v[164:167], v[12:15]\n\t"
      "ds_read_b128 v[60:63], v108 offset:50176\n\t"
      "buffer_store_dwordx4 v[88:91], v107, s[64:67], s75 offen sc1\n\t"
      "s_add_u32 s75, s75, 2097152\n\t"
      "s_waitcnt lgkmcnt(7)\n\t"
      "v_mfma_f32_16x16x32_bf16 v[16:19], v[64:67], v[160:163], v[16:19]\n\t"
      "v_mfma_f32_16x16x32_bf16 v[20:23], v[64:67], v[164:167], v[20:23]\n\t"
      "ds_read_b128 v[64:67], v108 offset:51200\n\t"
      "s_waitcnt lgkmcnt(7)\n\t"
      "v_mfma_f32_16x16x32_bf16 v[24:27], v[68:71], v[160:163], v[24:27]\n\t"
      "v_mfma_f32_16x16x32_bf16 v[28:31], v[68:71], v[164:167], v[28:31]\n\t"
      "ds_read_b128 v[68:71], v108 offset:52224\n\t"
      "s_add_u32 s15, s18, 15\n\t"
      "s_and_b32 s15, s15, 15\n\t"
      "s_mul_i32 s15, s15, 28672\n\t"
      "s_add_u32 m0, s34, 20480\n\t"
      "s_add_u32 s15, s15, s61\n\t"
      "buffer_load_dwordx4 v107, s[64:67], s15 offen  lds\n\t"
      "s_waitcnt lgkmcnt(7)\n\t"
      "v_mfma_f32_16x16x32_bf16 v[0:3], v[72:75], v[168:171], v[0:3]\n\t"
      "v_mfma_f32_16x16x32_bf16 v[4:7], v[72:75], v[172:175], v[4:7]\n\t"
      "ds_read_b128 v[72:75], v108 offset:53248\n\t"
      "s_waitcnt lgkmcnt(7)\n\t"
      "v_mfma_f32_16x16x32_bf16 v[8:11], v[76:79], v[168:171], v[8:11]\n\t"
      "v_mfma_f32_16x16x32_bf16 v[12:15], v[76:79], v[172:175], v[12:15]\n\t"
      "ds_read_b128 v[76:79], v108 offset:54272\n\t"
      "s_cmp_lt_u32 s40, 5\n\t"
      "s_cbranch_scc1 L_ism_%=\n\t"
      "s_lshl_b32 s54, s43, 7\n\t"
      "s_add_u32 s55, s54, 131072\n\t"
      "s_add_u32 s54, s54, 65536\n\t"
      "s_cmp_eq_u32 s40, 5\n\t"
      "s_cselect_b32 s54, s55, s54\n\t"
      "s_lshl_b32 s55, s43, 8\n\t"
      "s_add_u32 s55, s55, 98304\n\t"
      "s_cmp_lt_u32 s43, 112\n\t"
      "s_cselect_b32 s55, s55, 83886080\n\t"
      "s_cmp_lt_u32 s40, 7\n\t"
      "s_cselect_b32 s54, s54, s55\n\t"
      "s_mov_b32 s55, 0xf0\n\t"
      "s_cselect_b32 s55, 0x70, s55\n\t"
      "v_and_b32 v92, s55, v107\n\t"
      "buffer_store_dwordx4 v[88:91], v92, s[64:67], s54 offen sc1\n\t"
      "L_ism_%=:\n\t"
      "s_waitcnt lgkmcnt(7)\n\t"
      "v_mfma_f32_16x16x32_bf16 v[16:19], v[80:83], v[168:171], v[16:19]\n\t"
      "v_mfma_f32_16x16x32_bf16 v[20:23], v[80:83], v[172:175], v[20:23]\n\t"
      "ds_read_b128 v[80:83], v108 offset:55296\n\t"
      "s_waitcnt lgkmcnt(7)\n\t"
      "v_mfma_f32_16x16x32_bf16 v[24:27], v[84:87], v[168:171], v[24:27]\n\t"
      "v_mfma_f32_16x16x32_bf16 v[28:31], v[84:87], v[172:175], v[28:31]\n\t"
      "ds_read_b128 v[84:87], v108 offset:56320\n\t"
      "s_load_dwordx4 s[20:23], %[ka], 0x40\n\t"
      "s_load_dwordx2 s[28:29], %[ka], 0x50\n\t"
      "s_waitcnt lgkmcnt(0)\n\t"
      "s_mov_b32 s24, s22\n\t"
      "s_and_b32 s25, s23, 0xffff\n\t"
      "s_and_b32 s21, s21, 0xffff\n\t"
      "s_and_b32 s29, s29, 0xffff\n\t"
      "s_cmp_lt_i32 s10, 0\n\t"
      "s_cselect_b32 s22, 0, 1605632\n\t"
      "s_mov_b32 s26, s22\n\t"
      "s_mov_b32 s23, 0x20000\n\t"
      "s_mov_b32 s27, 0x20000\n\t"
      "s_mov_b32 s30, 1605632\n\t"
      "s_mov_b32 s31, 0x20000\n\t"
      "v_lshrrev_b32 v120, 1, v104\n\t"
      "v_and_b32 v121, 1, v104\n\t"
      "v_lshlrev_b32 v121, 4, v121\n\t"
      "v_mul_u32_u24 v110, 3584, v120\n\t"
      "v_add_u32 v110, v110, v121\n\t"
      "v_add_u32 v110, s10, v110\n\t"
      "v_mul_u32_u24 v111, 896, v120\n\t"
      "v_add_u32 v111, v111, v121\n\t"
      "v_add_u32 v111, s11, v111\n\t"
      "s_waitcnt vmcnt(10)\n\t"
      "s_barrier\n\t"
      "s_waitcnt lgkmcnt(7)\n\t"
      "v_mfma_f32_16x16x32_bf16 v[0:3], v[56:59], v[176:179], v[0:3]\n\t"
      "v_mfma_f32_16x16x32_bf16 v[4:7], v[56:59], v[180:183], v[4:7]\n\t"
      "ds_read_b128 v[56:59], v109 offset:0\n\t"
      "s_add_u32 s15, s16, 0\n\t"
      "s_and_b32 s15, s15, 15\n\t"
      "s_mul_i32 s15, s15, 28672\n\t"
      "s_add_u32 m0, s34, 32768\n\t"
      "s_add_u32 s15, s15, s62\n\t"
      "s_add_u32 s15, s15, 6144\n\t"
      "buffer_load_dwordx4 v107, s[64:67], s15 offen  lds\n\t"
      "s_waitcnt lgkmcnt(7)\n\t"
      "v_mfma_f32_16x16x32_bf16 v[8:11], v[60:63], v[176:179], v[8:11]\n\t"
      "v_mfma_f32_16x16x32_bf16 v[12:15], v[60:63], v[180:183], v[12:15]\n\t"
      "ds_read_b128 v[60:63], v109 offset:1024\n\t"
      "s_cmp_lt_u32 s40, 4\n\t"
      "s_cbranch_scc0 L_mpf_%=\n\t"
      "s_cmp_lt_u32 s40, 2\n\t"
      "s_cbranch_scc0 L_mpfd_%=\n\t"
      "v_mul_u32_u24 v120, 4682, v104\n\t"
      "v_lshrrev_b32 v120, 16, v120\n\t"
      "v_mul_u32_u24 v121, 14, v120\n\t"
      "v_sub_u32 v121, v104, v121\n\t"
      "s_mul_i32 s54, s42, 14\n\t"
      "v_add_u32 v121, s54, v121\n\t"
      "v_mul_u32_u24 v121, 3584, v121\n\t"
      "v_lshlrev_b32 v120, 3, v120\n\t"
      "v_add_u32 v120, s41, v120\n\t"
      "v_cmp_gt_u32 vcc, 28, v120\n\t"
      "v_lshl_add_u32 v121, v120, 7, v121\n\t"
      "v_cmp_gt_u32 s[72:73], 56, v104\n\t"
      "s_cmp_eq_u32 s40, 0\n\t"
      "s_cselect_b64 s[68:69], s[20:21], s[24:25]\n\t"
      "s_mov_b32 s70, 1605632\n\t"
      "s_branch L_mpfj_%=\n\t"
      "L_mpfd_%=:\n\t"
      "s_sub_u32 s54, s40, 2\n\t"
      "s_lshl_b32 s54, s54, 2\n\t"
      "s_add_u32 s54, s54, s41\n\t"
      "s_sub_u32 s54, s54, 4\n\t"
      "s_mul_i32 s55, s42, 56\n\t"
      "v_add_u32 v121, s55, v104\n\t"
      "v_mul_u32_u24 v121, 896, v121\n\t"
      "s_lshl_b32 s55, s54, 7\n\t"
      "v_add_u32 v121, s55, v121\n\t"
      "v_cmp_gt_u32 vcc, 56, v104\n\t"
      "v_cmp_gt_u32 s[72:73], 56, v104\n\t"
      "s_mov_b64 s[68:69], s[28:29]\n\t"
      "s_cmp_lt_u32 s54, 7\n\t"
      "s_cselect_b32 s70, 1605632, 0\n\t"
      "s_cmp_ge_u32 s41, 4\n\t"
      "s_cselect_b32 s70, s70, 0\n\t"
      "L_mpfj_%=:\n\t"
      "s_and_b64 vcc, vcc, s[72:73]\n\t"
      "s_mov_b32 s71, 0x20000\n\t"
      "v_mov_b32 v120, 0x7ffffff0\n\t"
      "v_cndmask_b32 v121, v120, v121, vcc\n\t"
      "buffer_load_dword v103, v121, s[68:71], 0 offen\n\t"
      "L_mpf_%=:\n\t"
      "s_waitcnt lgkmcnt(7)\n\t"
      "v_mfma_f32_16x16x32_bf16 v[16:19], v[64:67], v[176:179], v[16:19]\n\t"
      "v_mfma_f32_16x16x32_bf16 v[20:23], v[64:67], v[180:183], v[20:23]\n\t"
      "ds_read_b128 v[64:67], v109 offset:2048\n\t"
      "s_waitcnt lgkmcnt(7)\n\t"
      "v_mfma_f32_16x16x32_bf16 v[24:27], v[68:71], v[176:179], v[24:27]\n\t"
      "v_mfma_f32_16x16x32_bf16 v[28:31], v[68:71], v[180:183], v[28:31]\n\t"
      "ds_read_b128 v[68:71], v109 offset:3072\n\t"
      "s_add_u32 s15, s16, 4\n\t"
      "s_and_b32 s15, s15, 15\n\t"
      "s_mul_i32 s15, s15, 28672\n\t"
      "s_add_u32 m0, s34, 36864\n\t"
      "s_add_u32 s15, s15, s62\n\t"
      "s_add_u32 s15, s15, 6144\n\t"
      "buffer_load_dwordx4 v107, s[64:67], s15 offen  lds\n\t"
      "s_waitcnt lgkmcnt(7)\n\t"
      "v_mfma_f32_16x16x32_bf16 v[0:3], v[72:75], v[184:187], v[0:3]\n\t"
      "v_mfma_f32_16x16x32_bf16 v[4:7], v[72:75], v[188:191], v[4:7]\n\t"
      "ds_read_b128 v[72:75], v109 offset:4096\n\t"
      "s_waitcnt lgkmcnt(7)\n\t"
      "v_mfma_f32_16x16x32_bf16 v[8:11], v[76:79], v[184:187], v[8:11]\n\t"
      "v_mfma_f32_16x16x32_bf16 v[12:15], v[76:79], v[188:191], v[12:15]\n\t"
      "ds_read_b128 v[76:79], v109 offset:5120\n\t"
      "s_waitcnt lgkmcnt(7)\n\t"
      "v_mfma_f32_16x16x32_bf16 v[16:19], v[80:83], v[184:187], v[16:19]\n\t"
      "v_mfma_f32_16x16x32_bf16 v[20:23], v[80:83], v[188:191], v[20:23]\n\t"
      "ds_read_b128 v[80:83], v109 offset:6144\n\t"
      "s_waitcnt lgkmcnt(7)\n\t"
      "v_mfma_f32_16x16x32_bf16 v[24:27], v[84:87], v[184:187], v[24:27]\n\t"
      "v_mfma_f32_16x16x32_bf16 v[28:31], v[84:87], v[188:191], v[28:31]\n\t"
      "ds_read_b128 v[84:87], v109 offset:7168\n\t"
      "s_waitcnt vmcnt(8)\n\t"
      "s_barrier\n\t"
      "s_waitcnt lgkmcnt(7)\n\t"
      "v_mfma_f32_16x16x32_bf16 v[0:3], v[56:59], v[192:195], v[0:3]\n\t"
      "v_mfma_f32_16x16x32_bf16 v[4:7], v[56:59], v[196:199], v[4:7]\n\t"
      "ds_read_b128 v[56:59], v109 offset:16384\n\t"
      "s_add_u32 s15, s16, 8\n\t"
      "s_and_b32 s15, s15, 15\n\t"
      "s_mul_i32 s15, s15, 28672\n\t"
      "s_add_u32 m0, s34, 49152\n\t"
      "s_add_u32 s15, s15, s62\n\t"
      "s_add_u32 s15, s15, 6144\n\t"
      "buffer_load_dwordx4 v107, s[64:67], s15 offen  lds\n\t"
      "s_waitcnt lgkmcnt(7)\n\t"
      "v_mfma_f32_16x16x32_bf16 v[8:11], v[60:63], v[192:195], v[8:11]\n\t"
      "v_mfma_f32_16x16x32_bf16 v[12:15], v[60:63], v[196:199], v[12:15]\n\t"
      "ds_read_b128 v[60:63], v109 offset:17408\n\t"
      "s_waitcnt lgkmcnt(7)\n\t"
      "v_mfma_f32_16x16x32_bf16 v[16:19], v[64:67], v[192:195], v[16:19]\n\t"
      "v_mfma_f32_16x16x32_bf16 v[20:23], v[64:67], v[196:199], v[20:23]\n\t"
      "ds_read_b128 v[64:67], v109 offset:18432\n\t"
      "s_waitcnt lgkmcnt(7)\n\t"
      "v_mfma_f32_16x16x32_bf16 v[24:27], v[68:71], v[192:195], v[24:27]\n\t"
      "v_mfma_f32_16x16x32_bf16 v[28:31], v[68:71], v[196:199], v[28:31]\n\t"
      "ds_read_b128 v[68:71], v109 offset:19456\n\t"
      "s_add_u32 s15, s16, 12\n\t"
      "s_and_b32 s15, s15, 15\n\t"
      "s_mul_i32 s15, s15, 28672\n\t"
      "s_add_u32 m0, s34, 53248\n\t"
      "s_add_u32 s15, s15, s62\n\t"
      "s_add_u32 s15, s15, 6144\n\t"
      "buffer_load_dwordx4 v107, s[64:67], s15 offen  lds\n\t"
      "s_waitcnt lgkmcnt(7)\n\t"
      "v_mfma_f32_16x16x32_bf16 v[0:3], v[72:75], v[200:203], v[0:3]\n\t"
      "v_mfma_f32_16x16x32_bf16 v[4:7], v[72:75], v[204:207], v[4:7]\n\t"
      "ds_read_b128 v[72:75], v109 offset:20480\n\t"
      "s_waitcnt lgkmcnt(7)\n\t"
      "v_mfma_f32_16x16x32_bf16 v[8:11], v[76:79], v[200:203], v[8:11]\n\t"
      "v_mfma_f32_16x16x32_bf16 v[12:15], v[76:79], v[204:207], v[12:15]\n\t"
      "ds_read_b128 v[76:79], v109 offset:21504\n\t"
      "s_waitcnt lgkmcnt(7)\n\t"
      "v_mfma_f32_16x16x32_bf16 v[16:19], v[80:83], v[200:203], v[16:19]\n\t"
      "v_mfma_f32_16x16x32_bf16 v[20:23], v[80:83], v[204:207], v[20:23]\n\t"
      "ds_read_b128 v[80:83], v109 offset:22528\n\t"
      "s_waitcnt lgkmcnt(7)\n\t"
      "v_mfma_f32_16x16x32_bf16 v[24:27], v[84:87], v[200:203], v[24:27]\n\t"
      "v_mfma_f32_16x16x32_bf16 v[28:31], v[84:87], v[204:207], v[28:31]\n\t"
      "ds_read_b128 v[84:87], v109 offset:23552\n\t"
      "s_waitcnt vmcnt(7)\n\t"
      "s_barrier\n\t"
      "s_waitcnt lgkmcnt(7)\n\t"
      "v_mfma_f32_16x16x32_bf16 v[0:3], v[56:59], v[208:211], v[0:3]\n\t"
      "v_mfma_f32_16x16x32_bf16 v[4:7], v[56:59], v[212:215], v[4:7]\n\t"
      "ds_read_b128 v[56:59], v108 offset:0\n\t"
      "s_add_u32 s15, s17, 0\n\t"
      "s_and_b32 s15, s15, 15\n\t"
      "s_mul_i32 s15, s15, 28672\n\t"
      "s_add_u32 m0, s34, 65536\n\t"
      "s_add_u32 s15, s15, s63\n\t"
      "s_add_u32 s15, s15, 4096\n\t"
      "buffer_load_dwordx4 v107, s[64:67], s15 offen  lds\n\t"
      "s_waitcnt lgkmcnt(7)\n\t"
      "v_mfma_f32_16x16x32_bf16 v[8:11], v[60:63], v[208:211], v[8:11]\n\t"
      "v_mfma_f32_16x16x32_bf16 v[12:15], v[60:63], v[212:215], v[12:15]\n\t"
      "ds_read_b128 v[60:63], v108 offset:1024\n\t"
      "s_waitcnt lgkmcnt(7)\n\t"
      "v_mfma_f32_16x16x32_bf16 v[16:19], v[64:67], v[208:211], v[16:19]\n\t"
      "v_mfma_f32_16x16x32_bf16 v[20:23], v[64:67], v[212:215], v[20:23]\n\t"
      "ds_read_b128 v[64:67], v108 offset:2048\n\t"
      "s_waitcnt lgkmcnt(7)\n\t"
      "v_mfma_f32_16x16x32_bf16 v[24:27], v[68:71], v[208:211], v[24:27]\n\t"
      "v_mfma_f32_16x16x32_bf16 v[28:31], v[68:71], v[212:215], v[28:31]\n\t"
      "ds_read_b128 v[68:71], v108 offset:3072\n\t"
      "s_add_u32 s15, s17, 2\n\t"
      "s_and_b32 s15, s15, 15\n\t"
      "s_mul_i32 s15, s15, 28672\n\t"
      "s_add_u32 m0, s34, 69632\n\t"
      "s_add_u32 s15, s15, s63\n\t"
      "s_add_u32 s15, s15, 4096\n\t"
      "buffer_load_dwordx4 v107, s[64:67], s15 offen  lds\n\t"
      "s_waitcnt lgkmcnt(7)\n\t"
      "v_mfma_f32_16x16x32_bf16 v[0:3], v[72:75], v[216:219], v[0:3]\n\t"
      "v_mfma_f32_16x16x32_bf16 v[4:7], v[72:75], v[220:223], v[4:7]\n\t"
      "ds_read_b128 v[72:75], v108 offset:4096\n\t"
      "s_waitcnt lgkmcnt(7)\n\t"
      "v_mfma_f32_16x16x32_bf16 v[8:11], v[76:79], v[216:219], v[8:11]\n\t"
      "v_mfma_f32_16x16x32_bf16 v[12:15], v[76:79], v[220:223], v[12:15]\n\t"
      "ds_read_b128 v[76:79], v108 offset:5120\n\t"
      "s_waitcnt lgkmcnt(7)\n\t"
      "v_mfma_f32_16x16x32_bf16 v[16:19], v[80:83], v[216:219], v[16:19]\n\t"
      "v_mfma_f32_16x16x32_bf16 v[20:23], v[80:83], v[220:223], v[20:23]\n\t"
      "ds_read_b128 v[80:83], v108 offset:6144\n\t"
      "s_waitcnt lgkmcnt(7)\n\t"
      "v_mfma_f32_16x16x32_bf16 v[24:27], v[84:87], v[216:219], v[24:27]\n\t"
      "v_mfma_f32_16x16x32_bf16 v[28:31], v[84:87], v[220:223], v[28:31]\n\t"
      "ds_read_b128 v[84:87], v108 offset:7168\n\t"
      "s_waitcnt vmcnt(6)\n\t"
      "s_barrier\n\t"
      "s_waitcnt lgkmcnt(7)\n\t"
      "v_mfma_f32_16x16x32_bf16 v[0:3], v[56:59], v[224:227], v[0:3]\n\t"
      "v_mfma_f32_16x16x32_bf16 v[4:7], v[56:59], v[228:231], v[4:7]\n\t"
      "ds_read_b128 v[56:59], v108 offset:16384\n\t"
      "s_add_u32 s15, s17, 4\n\t"
      "s_and_b32 s15, s15, 15\n\t"
      "s_mul_i32 s15, s15, 28672\n\t"
      "s_add_u32 m0, s34, 81920\n\t"
      "s_add_u32 s15, s15, s63\n\t"
      "s_add_u32 s15, s15, 4096\n\t"
      "buffer_load_dwordx4 v107, s[64:67], s15 offen  lds\n\t"
      "s_waitcnt lgkmcnt(7)\n\t"
      "v_mfma_f32_16x16x32_bf16 v[8:11], v[60:63], v[224:227], v[8:11]\n\t"
      "v_mfma_f32_16x16x32_bf16 v[12:15], v[60:63], v[228:231], v[12:15]\n\t"
      "ds_read_b128 v[60:63], v108 offset:17408\n\t"
      "s_waitcnt lgkmcnt(7)\n\t"
      "v_mfma_f32_16x16x32_bf16 v[16:19], v[64:67], v[224:227], v[16:19]\n\t"
      "v_mfma_f32_16x16x32_bf16 v[20:23], v[64:67], v[228:231], v[20:23]\n\t"
      "ds_read_b128 v[64:67], v108 offset:18432\n\t"
      "s_waitcnt lgkmcnt(7)\n\t"
      "v_mfma_f32_16x16x32_bf16 v[24:27], v[68:71], v[224:227], v[24:27]\n\t"
      "v_mfma_f32_16x16x32_bf16 v[28:31], v[68:71], v[228:231], v[28:31]\n\t"
      "ds_read_b128 v[68:71], v108 offset:19456\n\t"
      "s_add_u32 s15, s17, 6\n\t"
      "s_and_b32 s15, s15, 15\n\t"
      "s_mul_i32 s15, s15, 28672\n\t"
      "s_add_u32 m0, s34, 86016\n\t"
      "s_add_u32 s15, s15, s63\n\t"
      "s_add_u32 s15, s15, 4096\n\t"
      "buffer_load_dwordx4 v107, s[64:67], s15 offen  lds\n\t"
      "s_waitcnt lgkmcnt(7)\n\t"
      "v_mfma_f32_16x16x32_bf16 v[0:3], v[72:75], v[232:235], v[0:3]\n\t"
      "v_mfma_f32_16x16x32_bf16 v[4:7], v[72:75], v[236:239], v[4:7]\n\t"
      "ds_read_b128 v[72:75], v108 offset:20480\n\t"
      "s_waitcnt lgkmcnt(7)\n\t"
      "v_mfma_f32_16x16x32_bf16 v[8:11], v[76:79], v[232:235], v[8:11]\n\t"
      "v_mfma_f32_16x16x32_bf16 v[12:15], v[76:79], v[236:239], v[12:15]\n\t"
      "ds_read_b128 v[76:79], v108 offset:21504\n\t"
      "s_waitcnt lgkmcnt(7)\n\t"
      "v_mfma_f32_16x16x32_bf16 v[16:19], v[80:83], v[232:235], v[16:19]\n\t"
      "v_mfma_f32_16x16x32_bf16 v[20:23], v[80:83], v[236:239], v[20:23]\n\t"
      "ds_read_b128 v[80:83], v108 offset:22528\n\t"
      "s_waitcnt lgkmcnt(7)\n\t"
      "v_mfma_f32_16x16x32_bf16 v[24:27], v[84:87], v[232:235], v[24:27]\n\t"
      "v_mfma_f32_16x16x32_bf16 v[28:31], v[84:87], v[236:239], v[28:31]\n\t"
      "ds_read_b128 v[84:87], v108 offset:23552\n\t"
      "s_waitcnt vmcnt(6)\n\t"
      "s_barrier\n\t"
      "s_waitcnt lgkmcnt(7)\n\t"
      "v_mfma_f32_16x16x32_bf16 v[0:3], v[56:59], v[240:243], v[0:3]\n\t"
      "v_mfma_f32_16x16x32_bf16 v[4:7], v[56:59], v[244:247], v[4:7]\n\t"
      "ds_read_b128 v[56:59], v108 offset:32768\n\t"
      "s_add_u32 s15, s17, 8\n\t"
      "s_and_b32 s15, s15, 15\n\t"
      "s_mul_i32 s15, s15, 28672\n\t"
      "s_add_u32 m0, s34, 0\n\t"
      "s_add_u32 s15, s15, s63\n\t"
      "s_add_u32 s15, s15, 4096\n\t"
      "buffer_load_dwordx4 v107, s[64:67], s15 offen  lds\n\t"
      "s_waitcnt lgkmcnt(7)\n\t"
      "v_mfma_f32_16x16x32_bf16 v[8:11], v[60:63], v[240:243], v[8:11]\n\t"
      "v_mfma_f32_16x16x32_bf16 v[12:15], v[60:63], v[244:247], v[12:15]\n\t"
      "ds_read_b128 v[60:63], v108 offset:33792\n\t"
      "s_lshl_b32 s54, s40, 2\n\t"
      "s_add_u32 s54, s54, 0\n\t"
      "s_lshl_b32 s55, s54, 10\n\t"
      "s_add_u32 s55, s55, 131072\n\t"
      "s_cmp_lt_u32 s54, 28\n\t"
      "s_cselect_b32 s55, s55, 161024\n\t"
      "s_cselect_b32 s58, -1, 0\n\t"
      "s_mov_b32 m0, s55\n\t"
      "s_sub_u32 s55, s54, 14\n\t"
      "s_cmp_lt_u32 s54, 14\n\t"
      "s_cselect_b32 s54, s54, s55\n\t"
      "s_cselect_b64 s[68:69], s[20:21], s[24:25]\n\t"
      "s_and_b32 s70, s22, s58\n\t"
      "s_mov_b32 s71, 0x20000\n\t"
      "s_mul_i32 s54, s54, 114688\n\t"
      "buffer_load_dwordx4 v110, s[68:71], s54 offen lds\n\t"
      "s_cmp_eq_u32 s40, 0\n\t"
      "s_cbranch_scc0 L_f0p_%=\n\t"
      "v_mov_b32 v120, s44\n\t"
      "v_mov_b32 v121, s45\n\t"
      "s_lshl_b32 s54, s43, 3\n\t"
      "s_add_u32 s54, s54, 40960\n\t"
      "v_mov_b32 v116, 0\n\t"
      "s_mov_b64 exec, 1\n\t"
      "buffer_store_dwordx2 v[120:121], v116, s[64:67], s54 offen sc1\n\t"
      "s_mov_b64 exec, -1\n\t"
      "L_f0p_%=:\n\t"
      "s_waitcnt lgkmcnt(7)\n\t"
      "v_mfma_f32_16x16x32_bf16 v[16:19], v[64:67], v[240:243], v[16:19]\n\t"
      "v_mfma_f32_16x16x32_bf16 v[20:23], v[64:67], v[244:247], v[20:23]\n\t"
      "ds_read_b128 v[64:67], v108 offset:34816\n\t"
      "s_waitcnt lgkmcnt(7)\n\t"
      "v_mfma_f32_16x16x32_bf16 v[24:27], v[68:71], v[240:243], v[24:27]\n\t"
      "v_mfma_f32_16x16x32_bf16 v[28:31], v[68:71], v[244:247], v[28:31]\n\t"
      "ds_read_b128 v[68:71], v108 offset:35840\n\t"
      "s_add_u32 s15, s17, 10\n\t"
      "s_and_b32 s15, s15, 15\n\t"
      "s_mul_i32 s15, s15, 28672\n\t"
      "s_add_u32 m0, s34, 4096\n\t"
      "s_add_u32 s15, s15, s63\n\t"
      "s_add_u32 s15, s15, 4096\n\t"
      "buffer_load_dwordx4 v107, s[64:67], s15 offen  lds\n\t"
      "s_waitcnt lgkmcnt(7)\n\t"
      "v_mfma_f32_16x16x32_bf16 v[0:3], v[72:75], v[248:251], v[0:3]\n\t"
      "v_mfma_f32_16x16x32_bf16 v[4:7], v[72:75], v[252:255], v[4:7]\n\t"
      "ds_read_b128 v[72:75], v108 offset:36864\n\t"
      "s_waitcnt lgkmcnt(7)\n\t"
      "v_mfma_f32_16x16x32_bf16 v[8:11], v[76:79], v[248:251], v[8:11]\n\t"
      "v_mfma_f32_16x16x32_bf16 v[12:15], v[76:79], v[252:255], v[12:15]\n\t"
      "ds_read_b128 v[76:79], v108 offset:37888\n\t"
      "s_lshl_b32 s54, s40, 2\n\t"
      "s_add_u32 s54, s54, 1\n\t"
      "s_lshl_b32 s55, s54, 10\n\t"
      "s_add_u32 s55, s55, 131072\n\t"
      "s_cmp_lt_u32 s54, 28\n\t"
      "s_cselect_b32 s55, s55, 161024\n\t"
      "s_cselect_b32 s58, -1, 0\n\t"
      "s_mov_b32 m0, s55\n\t"
      "s_sub_u32 s55, s54, 14\n\t"
      "s_cmp_lt_u32 s54, 14\n\t"
      "s_cselect_b32 s54, s54, s55\n\t"
      "s_cselect_b64 s[68:69], s[20:21], s[24:25]\n\t"
      "s_and_b32 s70, s22, s58\n\t"
      "s_mov_b32 s71, 0x20000\n\t"
      "s_mul_i32 s54, s54, 114688\n\t"
      "buffer_load_dwordx4 v110, s[68:71], s54 offen lds\n\t"
      "s_waitcnt lgkmcnt(7)\n\t"
      "v_mfma_f32_16x16x32_bf16 v[16:19], v[80:83], v[248:251], v[16:19]\n\t"
      "v_mfma_f32_16x16x32_bf16 v[20:23], v[80:83], v[252:255], v[20:23]\n\t"
      "ds_read_b128 v[80:83], v108 offset:38912\n\t"
      "s_waitcnt lgkmcnt(7)\n\t"
      "v_mfma_f32_16x16x32_bf16 v[24:27], v[84:87], v[248:251], v[24:27]\n\t"
      "v_mfma_f32_16x16x32_bf16 v[28:31], v[84:87], v[252:255], v[28:31]\n\t"
      "ds_read_b128 v[84:87], v108 offset:39936\n\t"
      "s_waitcnt vmcnt(8)\n\t"
      "s_barrier\n\t"
      "s_waitcnt lgkmcnt(7)\n\t"
      "v_mfma_f32_16x16x32_bf16 v[48:51], v[56:59], v[128:131], 0\n\t"
      "v_mfma_f32_16x16x32_bf16 v[52:55], v[56:59], v[132:135], 0\n\t"
      "ds_read_b128 v[56:59], v108 offset:49152\n\t"
      "s_add_u32 s15, s17, 12\n\t"
      "s_and_b32 s15, s15, 15\n\t"
      "s_mul_i32 s15, s15, 28672\n\t"
      "s_add_u32 m0, s34, 16384\n\t"
      "s_add_u32 s15, s15, s63\n\t"
      "s_add_u32 s15, s15, 4096\n\t"
      "buffer_load_dwordx4 v107, s[64:67], s15 offen  lds\n\t"
      "ds_write_b128 v114, v[4:7] offset:0\n\t"
      "s_waitcnt lgkmcnt(8)\n\t"
      "v_mfma_f32_16x16x32_bf16 v[48:51], v[60:63], v[136:139], v[48:51]\n\t"
      "v_mfma_f32_16x16x32_bf16 v[52:55], v[60:63], v[140:143], v[52:55]\n\t"
      "ds_read_b128 v[60:63], v108 offset:50176\n\t"
      "s_lshl_b32 s54, s40, 2\n\t"
      "s_add_u32 s54, s54, 2\n\t"
      "s_lshl_b32 s55, s54, 10\n\t"
      "s_add_u32 s55, s55, 131072\n\t"
      "s_cmp_lt_u32 s54, 28\n\t"
      "s_cselect_b32 s55, s55, 161024\n\t"
      "s_cselect_b32 s58, -1, 0\n\t"
      "s_mov_b32 m0, s55\n\t"
      "s_sub_u32 s55, s54, 14\n\t"
      "s_cmp_lt_u32 s54, 14\n\t"
      "s_cselect_b32 s54, s54, s55\n\t"
      "s_cselect_b64 s[68:69], s[20:21], s[24:25]\n\t"
      "s_and_b32 s70, s22, s58\n\t"
      "s_mov_b32 s71, 0x20000\n\t"
      "s_mul_i32 s54, s54, 114688\n\t"
      "buffer_load_dwordx4 v110, s[68:71], s54 offen lds\n\t"
      "s_waitcnt lgkmcnt(8)\n\t"
      "v_mfma_f32_16x16x32_bf16 v[48:51], v[64:67], v[144:147], v[48:51]\n\t"
      "v_mfma_f32_16x16x32_bf16 v[52:55], v[64:67], v[148:151], v[52:55]\n\t"
      "ds_read_b128 v[64:67], v108 offset:51200\n\t"
      "ds_write_b128 v114, v[12:15] offset:1024\n\t"
      "s_waitcnt lgkmcnt(9)\n\t"
      "v_mfma_f32_16x16x32_bf16 v[48:51], v[68:71], v[152:155], v[48:51]\n\t"
      "v_mfma_f32_16x16x32_bf16 v[52:55], v[68:71], v[156:159], v[52:55]\n\t"
      "ds_read_b128 v[68:71], v108 offset:52224\n\t"
      "s_add_u32 s15, s17, 14\n\t"
      "s_and_b32 s15, s15, 15\n\t"
      "s_mul_i32 s15, s15, 28672\n\t"
      "s_add_u32 m0, s34, 20480\n\t"
      "s_add_u32 s15, s15, s63\n\t"
      "s_add_u32 s15, s15, 4096\n\t"
      "buffer_load_dwordx4 v107, s[64:67], s15 offen  lds\n\t"
      "s_waitcnt lgkmcnt(9)\n\t"
      "v_mfma_f32_16x16x32_bf16 v[48:51], v[72:75], v[160:163], v[48:51]\n\t"
      "v_mfma_f32_16x16x32_bf16 v[52:55], v[72:75], v[164:167], v[52:55]\n\t"
      "ds_read_b128 v[72:75], v108 offset:53248\n\t"
      "ds_write_b128 v114, v[20:23] offset:2048\n\t"
      "s_waitcnt lgkmcnt(10)\n\t"
      "v_mfma_f32_16x16x32_bf16 v[48:51], v[76:79], v[168:171], v[48:51]\n\t"
      "v_mfma_f32_16x16x32_bf16 v[52:55], v[76:79], v[172:175], v[52:55]\n\t"
      "ds_read_b128 v[76:79], v108 offset:54272\n\t"
      "s_lshl_b32 s54, s40, 2\n\t"
      "s_add_u32 s54, s54, 3\n\t"
      "s_lshl_b32 s55, s54, 10\n\t"
      "s_add_u32 s55, s55, 131072\n\t"
      "s_cmp_lt_u32 s54, 28\n\t"
      "s_cselect_b32 s55, s55, 161024\n\t"
      "s_cselect_b32 s58, -1, 0\n\t"
      "s_mov_b32 m0, s55\n\t"
      "s_sub_u32 s55, s54, 14\n\t"
      "s_cmp_lt_u32 s54, 14\n\t"
      "s_cselect_b32 s54, s54, s55\n\t"
      "s_cselect_b64 s[68:69], s[20:21], s[24:25]\n\t"
      "s_and_b32 s70, s22, s58\n\t"
      "s_mov_b32 s71, 0x20000\n\t"
      "s_mul_i32 s54, s54, 114688\n\t"
      "buffer_load_dwordx4 v110, s[68:71], s54 offen lds\n\t"
      "s_waitcnt lgkmcnt(10)\n\t"
      "v_mfma_f32_16x16x32_bf16 v[48:51], v[80:83], v[176:179], v[48:51]\n\t"
      "v_mfma_f32_16x16x32_bf16 v[52:55], v[80:83], v[180:183], v[52:55]\n\t"
      "ds_read_b128 v[80:83], v108 offset:55296\n\t"
      "ds_write_b128 v114, v[28:31] offset:3072\n\t"
      "s_waitcnt lgkmcnt(11)\n\t"
      "v_mfma_f32_16x16x32_bf16 v[48:51], v[84:87], v[184:187], v[48:51]\n\t"
      "v_mfma_f32_16x16x32_bf16 v[52:55], v[84:87], v[188:191], v[52:55]\n\t"
      "ds_read_b128 v[84:87], v108 offset:56320\n\t"
      "s_waitcnt lgkmcnt(1)\n\t"
      "s_waitcnt vmcnt(10)\n\t"
      "s_barrier\n\t"
      "s_waitcnt lgkmcnt(7)\n\t"
      "v_mfma_f32_16x16x32_bf16 v[48:51], v[56:59], v[192:195], v[48:51]\n\t"
      "v_mfma_f32_16x16x32_bf16 v[52:55], v[56:59], v[196:199], v[52:55]\n\t"
      "ds_read_b128 v[56:59], v109 offset:0\n\t"
      "ds_read_b128 v[88:91], v115 offset:0\n\t"
      "s_waitcnt lgkmcnt(8)\n\t"
      "v_mfma_f32_16x16x32_bf16 v[48:51], v[60:63], v[200:203], v[48:51]\n\t"
      "v_mfma_f32_16x16x32_bf16 v[52:55], v[60:63], v[204:207], v[52:55]\n\t"
      "ds_read_b128 v[60:63], v109 offset:1024\n\t"
      "s_waitcnt lgkmcnt(8)\n\t"
      "v_mfma_f32_16x16x32_bf16 v[48:51], v[64:67], v[208:211], v[48:51]\n\t"
      "v_mfma_f32_16x16x32_bf16 v[52:55], v[64:67], v[212:215], v[52:55]\n\t"
      "ds_read_b128 v[64:67], v109 offset:2048\n\t"
      "ds_read_b128 v[92:95], v115 offset:1024\n\t"
      "s_waitcnt lgkmcnt(9)\n\t"
      "v_mfma_f32_16x16x32_bf16 v[48:51], v[68:71], v[216:219], v[48:51]\n\t"
      "v_mfma_f32_16x16x32_bf16 v[52:55], v[68:71], v[220:223], v[52:55]\n\t"
      "ds_read_b128 v[68:71], v109 offset:3072\n\t"
      "s_waitcnt lgkmcnt(9)\n\t"
      "v_mfma_f32_16x16x32_bf16 v[48:51], v[72:75], v[224:227], v[48:51]\n\t"
      "v_mfma_f32_16x16x32_bf16 v[52:55], v[72:75], v[228:231], v[52:55]\n\t"
      "ds_read_b128 v[72:75], v109 offset:4096\n\t"
      "ds_read_b128 v[96:99], v115 offset:2048\n\t"
      "s_waitcnt lgkmcnt(10)\n\t"
      "v_mfma_f32_16x16x32_bf16 v[48:51], v[76:79], v[232:235], v[48:51]\n\t"
      "v_mfma_f32_16x16x32_bf16 v[52:55], v[76:79], v[236:239], v[52:55]\n\t"
      "ds_read_b128 v[76:79], v109 offset:5120\n\t"
      "s_load_dwordx2 s[20:21], %[ka], 0x0\n\t"
      "s_load_dwordx4 s[60:63], %[ka], 0x30\n\t"
      "s_waitcnt lgkmcnt(0)\n\t"
      "s_and_b32 s21, s21, 0xffff\n\t"
      "s_mov_b32 s22, 1835008\n\t"
      "s_mov_b32 s23, 0x20000\n\t"
      "s_mov_b32 s36, s60\n\t"
      "s_and_b32 s37, s61, 0xffff\n\t"
      "s_mov_b32 s38, 896\n\t"
      "s_mov_b32 s39, 0x20000\n\t"
      "s_mov_b32 s24, s62\n\t"
      "s_and_b32 s25, s63, 0xffff\n\t"
      "s_mov_b32 s26, 1835008\n\t"
      "s_mov_b32 s27, 0x20000\n\t"
      "s_lshl_b32 s46, s43, 3\n\t"
      "s_add_u32 s46, s46, s40\n\t"
      "s_lshl_b32 s61, s46, 10\n\t"
      "s_add_u32 s61, s61, 5242880\n\t"
      "s_add_u32 s62, s61, 2097152\n\t"
      "s_add_u32 s63, s62, 2097152\n\t"
      "s_add_u32 s58, s63, 2097152\n\t"
      "v_cmp_gt_u32 s[72:73], 56, v104\n\t"
      "s_waitcnt lgkmcnt(10)\n\t"
      "v_mfma_f32_16x16x32_bf16 v[48:51], v[80:83], v[240:243], v[48:51]\n\t"
      "v_mfma_f32_16x16x32_bf16 v[52:55], v[80:83], v[244:247], v[52:55]\n\t"
      "ds_read_b128 v[80:83], v109 offset:6144\n\t"
      "ds_read_b128 v[100:103], v115 offset:3072\n\t"
      "s_waitcnt lgkmcnt(11)\n\t"
      "v_mfma_f32_16x16x32_bf16 v[48:51], v[84:87], v[248:251], v[48:51]\n\t"
      "v_mfma_f32_16x16x32_bf16 v[52:55], v[84:87], v[252:255], v[52:55]\n\t"
      "ds_read_b128 v[84:87], v109 offset:7168\n\t"
      "s_waitcnt lgkmcnt(1)\n\t"
      "s_waitcnt vmcnt(8)\n\t"
      "s_barrier\n\t"
      "s_waitcnt lgkmcnt(7)\n\t"
      "v_mfma_f32_16x16x32_bf16 v[32:35], v[56:59], v[128:131], 0\n\t"
      "v_mfma_f32_16x16x32_bf16 v[36:39], v[56:59], v[132:135], 0\n\t"
      "ds_read_b128 v[56:59], v109 offset:16384\n\t"
      "v_add_f32 v88, v0, v88\n\t"
      "v_add_f32 v89, v1, v89\n\t"
      "v_add_f32 v90, v2, v90\n\t"
      "v_add_f32 v91, v3, v91\n\t"
      "s_waitcnt lgkmcnt(7)\n\t"
      "v_mfma_f32_16x16x32_bf16 v[40:43], v[60:63], v[128:131], 0\n\t"
      "v_mfma_f32_16x16x32_bf16 v[44:47], v[60:63], v[132:135], 0\n\t"
      "ds_read_b128 v[60:63], v109 offset:17408\n\t"
      "ds_write_b128 v114, v[52:55] offset:0\n\t"
      "s_waitcnt lgkmcnt(8)\n\t"
      "v_mfma_f32_16x16x32_bf16 v[32:35], v[64:67], v[136:139], v[32:35]\n\t"
      "v_mfma_f32_16x16x32_bf16 v[36:39], v[64:67], v[140:143], v[36:39]\n\t"
      "ds_read_b128 v[64:67], v109 offset:18432\n\t"
      "v_add_f32 v92, v8, v92\n\t"
      "v_add_f32 v93, v9, v93\n\t"
      "v_add_f32 v94, v10, v94\n\t"
      "v_add_f32 v95, v11, v95\n\t"
      "s_waitcnt lgkmcnt(8)\n\t"
      "v_mfma_f32_16x16x32_bf16 v[40:43], v[68:71], v[136:139], v[40:43]\n\t"
      "v_mfma_f32_16x16x32_bf16 v[44:47], v[68:71], v[140:143], v[44:47]\n\t"
      "ds_read_b128 v[68:71], v109 offset:19456\n\t"
      "v_lshlrev_b32 v116, 3, v105\n\t"
      "s_lshl_b32 s74, s51, 7\n\t"
      "s_add_u32 s74, s74, 40960\n\t"
      "buffer_load_dwordx2 v[118:119], v116, s[64:67], s74 offen sc1\n\t"
      "s_waitcnt lgkmcnt(8)\n\t"
      "v_mfma_f32_16x16x32_bf16 v[32:35], v[72:75], v[144:147], v[32:35]\n\t"
      "v_mfma_f32_16x16x32_bf16 v[36:39], v[72:75], v[148:151], v[36:39]\n\t"
      "ds_read_b128 v[72:75], v109 offset:20480\n\t"
      "v_add_f32 v96, v16, v96\n\t"
      "v_add_f32 v97, v17, v97\n\t"
      "v_add_f32 v98, v18, v98\n\t"
      "v_add_f32 v99, v19, v99\n\t"
      "s_waitcnt lgkmcnt(8)\n\t"
      "v_mfma_f32_16x16x32_bf16 v[40:43], v[76:79], v[144:147], v[40:43]\n\t"
      "v_mfma_f32_16x16x32_bf16 v[44:47], v[76:79], v[148:151], v[44:47]\n\t"
      "ds_read_b128 v[76:79], v109 offset:21504\n\t"
      "s_waitcnt lgkmcnt(8)\n\t"
      "v_mfma_f32_16x16x32_bf16 v[32:35], v[80:83], v[152:155], v[32:35]\n\t"
      "v_mfma_f32_16x16x32_bf16 v[36:39], v[80:83], v[156:159], v[36:39]\n\t"
      "ds_read_b128 v[80:83], v109 offset:22528\n\t"
      "v_add_f32 v100, v24, v100\n\t"
      "v_add_f32 v101, v25, v101\n\t"
      "v_add_f32 v102, v26, v102\n\t"
      "v_add_f32 v103, v27, v103\n\t"
      "s_waitcnt lgkmcnt(8)\n\t"
      "v_mfma_f32_16x16x32_bf16 v[40:43], v[84:87], v[152:155], v[40:43]\n\t"
      "v_mfma_f32_16x16x32_bf16 v[44:47], v[84:87], v[156:159], v[44:47]\n\t"
      "ds_read_b128 v[84:87], v109 offset:23552\n\t"
      "s_waitcnt lgkmcnt(6)\n\t"
      "s_waitcnt vmcnt(6)\n\t"
      "s_barrier\n\t"
      "s_waitcnt lgkmcnt(7)\n\t"
      "v_mfma_f32_16x16x32_bf16 v[32:35], v[56:59], v[160:163], v[32:35]\n\t"
      "v_mfma_f32_16x16x32_bf16 v[36:39], v[56:59], v[164:167], v[36:39]\n\t"
      "ds_read_b128 v[56:59], v108 offset:0\n\t"
      "v_cvt_pk_bf16_f32 v88, v88, v89\n\t"
      "v_cvt_pk_bf16_f32 v89, v90, v91\n\t"
      "v_min_u32 v88, 0xfffffffe, v88\n\t"
      "v_min_u32 v89, 0xfffffffe, v89\n\t"
      "v_cvt_pk_bf16_f32 v90, v92, v93\n\t"
      "v_cvt_pk_bf16_f32 v91, v94, v95\n\t"
      "v_min_u32 v90, 0xfffffffe, v90\n\t"
      "v_min_u32 v91, 0xfffffffe, v91\n\t"
      "s_waitcnt lgkmcnt(7)\n\t"
      "v_mfma_f32_16x16x32_bf16 v[40:43], v[60:63], v[160:163], v[40:43]\n\t"
      "v_mfma_f32_16x16x32_bf16 v[44:47], v[60:63], v[164:167], v[44:47]\n\t"
      "ds_read_b128 v[60:63], v108 offset:1024\n\t"
      "s_waitcnt lgkmcnt(7)\n\t"
      "v_mfma_f32_16x16x32_bf16 v[32:35], v[64:67], v[168:171], v[32:35]\n\t"
      "v_mfma_f32_16x16x32_bf16 v[36:39], v[64:67], v[172:175], v[36:39]\n\t"
      "ds_read_b128 v[64:67], v108 offset:2048\n\t"
      "v_cvt_pk_bf16_f32 v92, v96, v97\n\t"
      "v_cvt_pk_bf16_f32 v93, v98, v99\n\t"
      "v_min_u32 v92, 0xfffffffe, v92\n\t"
      "v_min_u32 v93, 0xfffffffe, v93\n\t"
      "v_cvt_pk_bf16_f32 v94, v100, v101\n\t"
      "v_cvt_pk_bf16_f32 v95, v102, v103\n\t"
      "v_min_u32 v94, 0xfffffffe, v94\n\t"
      "v_min_u32 v95, 0xfffffffe, v95\n\t"
      "s_waitcnt lgkmcnt(7)\n\t"
      "v_mfma_f32_16x16x32_bf16 v[40:43], v[68:71], v[168:171], v[40:43]\n\t"
      "v_mfma_f32_16x16x32_bf16 v[44:47], v[68:71], v[172:175], v[44:47]\n\t"
      "ds_read_b128 v[68:71], v108 offset:3072\n\t"
      "s_waitcnt lgkmcnt(7)\n\t"
      "v_mfma_f32_16x16x32_bf16 v[32:35], v[72:75], v[176:179], v[32:35]\n\t"
      "v_mfma_f32_16x16x32_bf16 v[36:39], v[72:75], v[180:183], v[36:39]\n\t"
      "ds_read_b128 v[72:75], v108 offset:4096\n\t"
      "ds_read_b128 v[96:99], v115 offset:0\n\t"
      "s_waitcnt lgkmcnt(8)\n\t"
      "v_mfma_f32_16x16x32_bf16 v[40:43], v[76:79], v[176:179], v[40:43]\n\t"
      "v_mfma_f32_16x16x32_bf16 v[44:47], v[76:79], v[180:183], v[44:47]\n\t"
      "ds_read_b128 v[76:79], v108 offset:5120\n\t"
      "s_waitcnt lgkmcnt(8)\n\t"
      "v_mfma_f32_16x16x32_bf16 v[32:35], v[80:83], v[184:187], v[32:35]\n\t"
      "v_mfma_f32_16x16x32_bf16 v[36:39], v[80:83], v[188:191], v[36:39]\n\t"
      "ds_read_b128 v[80:83], v108 offset:6144\n\t"
      "v_cndmask_b32 v139, 0, v107, s[72:73]\n\t"
      "v_lshrrev_b32 v138, 4, v139\n\t"
      "v_mul_u32_u24 v138, 4682, v138\n\t"
      "v_lshrrev_b32 v138, 16, v138\n\t"
      "v_lshl_add_u32 v138, v138, 5, v139\n\t"
      "s_mul_i32 s54, s46, 896\n\t"
      "buffer_load_dwordx4 v[128:131], v139, s[20:23], s54 offen\n\t"
      "buffer_load_dwordx4 v[132:135], v139, s[36:39], 0 offen\n\t"
      "s_and_b32 s54, s40, 3\n\t"
      "s_lshl_b32 s54, s54, 9\n\t"
      "s_add_u32 s54, s54, 40960\n\t"
      "v_lshlrev_b32 v120, 3, v104\n\t"
      "buffer_load_dwordx2 v[136:137], v120, s[64:67], s54 offen sc1\n\t"
      "s_waitcnt lgkmcnt(8)\n\t"
      "v_mfma_f32_16x16x32_bf16 v[40:43], v[84:87], v[184:187], v[40:43]\n\t"
      "v_mfma_f32_16x16x32_bf16 v[44:47], v[84:87], v[188:191], v[44:47]\n\t"
      "ds_read_b128 v[84:87], v108 offset:7168\n\t"
      "s_waitcnt lgkmcnt(3)\n\t"
      "s_waitcnt vmcnt(5)\n\t"
      "s_barrier\n\t"
      "s_waitcnt lgkmcnt(7)\n\t"
      "v_mfma_f32_16x16x32_bf16 v[32:35], v[56:59], v[192:195], v[32:35]\n\t"
      "v_mfma_f32_16x16x32_bf16 v[36:39], v[56:59], v[196:199], v[36:39]\n\t"
      "ds_read_b128 v[56:59], v108 offset:16384\n\t"
      "v_add_f32 v96, v48, v96\n\t"
      "v_add_f32 v97, v49, v97\n\t"
      "v_add_f32 v98, v50, v98\n\t"
      "v_add_f32 v99, v51, v99\n\t"
      "s_waitcnt lgkmcnt(7)\n\t"
      "v_mfma_f32_16x16x32_bf16 v[40:43], v[60:63], v[192:195], v[40:43]\n\t"
      "v_mfma_f32_16x16x32_bf16 v[44:47], v[60:63], v[196:199], v[44:47]\n\t"
      "ds_read_b128 v[60:63], v108 offset:17408\n\t"
      "v_cvt_pk_bf16_f32 v100, v96, v97\n\t"
      "v_cvt_pk_bf16_f32 v101, v98, v99\n\t"
      "v_min_u32 v100, 0xfffffffe, v100\n\t"
      "v_min_u32 v101, 0xfffffffe, v101\n\t"
      "s_waitcnt lgkmcnt(7)\n\t"
      "v_mfma_f32_16x16x32_bf16 v[32:35], v[64:67], v[200:203], v[32:35]\n\t"
      "v_mfma_f32_16x16x32_bf16 v[36:39], v[64:67], v[204:207], v[36:39]\n\t"
      "ds_read_b128 v[64:67], v108 offset:18432\n\t"
      "s_waitcnt vmcnt(3)\n\t"
      "s_mov_b32 s14, 0\n\t"
      "L_f0c_%=:\n\t"
      "v_cmp_ne_u32 vcc, s44, v118\n\t"
      "v_cmp_ne_u32 s[68:69], s45, v119\n\t"
      "s_nop 1\n\t"
      "s_or_b64 vcc, vcc, s[68:69]\n\t"
      "s_cbranch_scc0 L_f0x_%=\n\t"
      "s_add_u32 s14, s14, 1\n\t"
      "s_cmp_lt_u32 s14, 0x40000\n\t"
      "s_cbranch_scc0 L_f0x_%=\n\t"
      "s_sleep 2\n\t"
      "buffer_load_dwordx2 v[118:119], v116, s[64:67], s74 offen sc1\n\t"
      "s_waitcnt vmcnt(0)\n\t"
      "s_branch L_f0c_%=\n\t"
      "L_f0x_%=:\n\t"
      "s_waitcnt lgkmcnt(7)\n\t"
      "v_mfma_f32_16x16x32_bf16 v[40:43], v[68:71], v[200:203], v[40:43]\n\t"
      "v_mfma_f32_16x16x32_bf16 v[44:47], v[68:71], v[204:207], v[44:47]\n\t"
      "ds_read_b128 v[68:71], v108 offset:19456\n\t"
      "buffer_store_dwordx4 v[88:91], v112, s[64:67], s35 offen offset:0 sc1\n\t"
      "s_waitcnt lgkmcnt(7)\n\t"
      "v_mfma_f32_16x16x32_bf16 v[32:35], v[72:75], v[208:211], v[32:35]\n\t"
      "v_mfma_f32_16x16x32_bf16 v[36:39], v[72:75], v[212:215], v[36:39]\n\t"
      "ds_read_b128 v[72:75], v108 offset:20480\n\t"
      "s_waitcnt lgkmcnt(7)\n\t"
      "v_mfma_f32_16x16x32_bf16 v[40:43], v[76:79], v[208:211], v[40:43]\n\t"
      "v_mfma_f32_16x16x32_bf16 v[44:47], v[76:79], v[212:215], v[44:47]\n\t"
      "ds_read_b128 v[76:79], v108 offset:21504\n\t"
      "buffer_store_dwordx4 v[92:95], v112, s[64:67], s35 offen offset:64 sc1\n\t"
      "s_waitcnt lgkmcnt(7)\n\t"
      "v_mfma_f32_16x16x32_bf16 v[32:35], v[80:83], v[216:219], v[32:35]\n\t"
      "v_mfma_f32_16x16x32_bf16 v[36:39], v[80:83], v[220:223], v[36:39]\n\t"
      "ds_read_b128 v[80:83], v108 offset:22528\n\t"
      "s_waitcnt lgkmcnt(7)\n\t"
      "v_mfma_f32_16x16x32_bf16 v[40:43], v[84:87], v[216:219], v[40:43]\n\t"
      "v_mfma_f32_16x16x32_bf16 v[44:47], v[84:87], v[220:223], v[44:47]\n\t"
      "ds_read_b128 v[84:87], v108 offset:23552\n\t"
      "buffer_store_dwordx2 v[100:101], v113, s[64:67], s35 offen offset:192 sc1\n\t"
      "s_barrier\n\t"
      "s_waitcnt lgkmcnt(7)\n\t"
      "v_mfma_f32_16x16x32_bf16 v[32:35], v[56:59], v[224:227], v[32:35]\n\t"
      "v_mfma_f32_16x16x32_bf16 v[36:39], v[56:59], v[228:231], v[36:39]\n\t"
      "s_waitcnt lgkmcnt(6)\n\t"
      "v_mfma_f32_16x16x32_bf16 v[40:43], v[60:63], v[224:227], v[40:43]\n\t"
      "v_mfma_f32_16x16x32_bf16 v[44:47], v[60:63], v[228:231], v[44:47]\n\t"
      "s_waitcnt lgkmcnt(5)\n\t"
      "v_mfma_f32_16x16x32_bf16 v[32:35], v[64:67], v[232:235], v[32:35]\n\t"
      "v_mfma_f32_16x16x32_bf16 v[36:39], v[64:67], v[236:239], v[36:39]\n\t"
      "s_waitcnt lgkmcnt(4)\n\t"
      "v_mfma_f32_16x16x32_bf16 v[40:43], v[68:71], v[232:235], v[40:43]\n\t"
      "v_mfma_f32_16x16x32_bf16 v[44:47], v[68:71], v[236:239], v[44:47]\n\t"
      "s_waitcnt lgkmcnt(3)\n\t"
      "v_mfma_f32_16x16x32_bf16 v[32:35], v[72:75], v[240:243], v[32:35]\n\t"
      "v_mfma_f32_16x16x32_bf16 v[36:39], v[72:75], v[244:247], v[36:39]\n\t"
      "s_waitcnt lgkmcnt(2)\n\t"
      "v_mfma_f32_16x16x32_bf16 v[40:43], v[76:79], v[240:243], v[40:43]\n\t"
      "v_mfma_f32_16x16x32_bf16 v[44:47], v[76:79], v[244:247], v[44:47]\n\t"
      "s_waitcnt lgkmcnt(1)\n\t"
      "v_mfma_f32_16x16x32_bf16 v[32:35], v[80:83], v[248:251], v[32:35]\n\t"
      "v_mfma_f32_16x16x32_bf16 v[36:39], v[80:83], v[252:255], v[36:39]\n\t"
      "s_waitcnt lgkmcnt(0)\n\t"
      "v_mfma_f32_16x16x32_bf16 v[40:43], v[84:87], v[248:251], v[40:43]\n\t"
      "v_mfma_f32_16x16x32_bf16 v[44:47], v[84:87], v[252:255], v[44:47]\n\t"
      "s_nop 7\n\t"
      "ds_write_b128 v114, v[36:39] offset:0\n\t"
      "ds_write_b128 v114, v[44:47] offset:1024\n\t"
      "s_waitcnt lgkmcnt(0)\n\t"
      "s_barrier\n\t"
      "ds_read_b128 v[96:99], v115 offset:0\n\t"
      "ds_read_b128 v[100:103], v115 offset:1024\n\t"
      "s_waitcnt lgkmcnt(1)\n\t"
      "v_add_f32 v96, v32, v96\n\t"
      "v_add_f32 v97, v33, v97\n\t"
      "v_add_f32 v98, v34, v98\n\t"
      "v_add_f32 v99, v35, v99\n\t"
      "s_waitcnt lgkmcnt(0)\n\t"
      "v_add_f32 v100, v40, v100\n\t"
      "v_add_f32 v101, v41, v101\n\t"
      "v_add_f32 v102, v42, v102\n\t"
      "v_add_f32 v103, v43, v103\n\t"
      "v_cvt_pk_bf16_f32 v96, v96, v97\n\t"
      "v_cvt_pk_bf16_f32 v97, v98, v99\n\t"
      "v_min_u32 v96, 0xfffffffe, v96\n\t"
      "v_min_u32 v97, 0xfffffffe, v97\n\t"
      "v_cvt_pk_bf16_f32 v98, v100, v101\n\t"
      "v_cvt_pk_bf16_f32 v99, v102, v103\n\t"
      "v_min_u32 v98, 0xfffffffe, v98\n\t"
      "v_min_u32 v99, 0xfffffffe, v99\n\t"
      "buffer_store_dwordx4 v[96:99], v112, s[64:67], s35 offen offset:128 sc1\n\t"
      "s_waitcnt vmcnt(0)\n\t"
      "buffer_load_dwordx4 v[140:143], v138, s[64:67], s61 offen sc1\n\t"
      "buffer_load_dwordx4 v[144:147], v138, s[64:67], s62 offen sc1\n\t"
      "buffer_load_dwordx4 v[148:151], v138, s[64:67], s63 offen sc1\n\t"
      "buffer_load_dwordx4 v[152:155], v138, s[64:67], s58 offen sc1\n\t"
      "s_mov_b32 s14, 0\n\t"
      "L_pp_%=:\n\t"
      "s_waitcnt vmcnt(0)\n\t"
      "v_max3_u32 v160, v140, v141, v142\n\t"
      "v_max3_u32 v160, v143, v144, v160\n\t"
      "v_max3_u32 v160, v145, v146, v160\n\t"
      "v_max3_u32 v160, v147, v148, v160\n\t"
      "v_max3_u32 v160, v149, v150, v160\n\t"
      "v_max3_u32 v160, v151, v152, v160\n\t"
      "v_max3_u32 v160, v153, v154, v160\n\t"
      "v_max_u32 v160, v155, v160\n\t"
      "v_cmp_eq_u32 vcc, -1, v160\n\t"
      "s_cbranch_vccz L_ppx_%=\n\t"
      "s_add_u32 s14, s14, 1\n\t"
      "s_cmp_lt_u32 s14, 65536\n\t"
      "s_cbranch_scc0 L_ppx_%=\n\t"
      "s_mov_b64 exec, vcc\n\t"
      "buffer_load_dwordx4 v[140:143], v138, s[64:67], s61 offen sc1\n\t"
      "buffer_load_dwordx4 v[144:147], v138, s[64:67], s62 offen sc1\n\t"
      "buffer_load_dwordx4 v[148:151], v138, s[64:67], s63 offen sc1\n\t"
      "buffer_load_dwordx4 v[152:155], v138, s[64:67], s58 offen sc1\n\t"
      "s_mov_b64 exec, -1\n\t"
      "s_branch L_pp_%=\n\t"
      "L_ppx_%=:\n\t"
      "v_lshlrev_b32 v160, 16, v140\n\t"
      "v_and_b32 v161, 0xffff0000, v140\n\t"
      "v_lshlrev_b32 v162, 16, v144\n\t"
      "v_and_b32 v163, 0xffff0000, v144\n\t"
      "v_lshlrev_b32 v164, 16, v148\n\t"
      "v_and_b32 v165, 0xffff0000, v148\n\t"
      "v_lshlrev_b32 v166, 16, v152\n\t"
      "v_and_b32 v167, 0xffff0000, v152\n\t"
      "v_pk_add_f32 v[160:161], v[160:161], v[162:163]\n\t"
      "v_pk_add_f32 v[164:165], v[164:165], v[166:167]\n\t"
      "v_lshlrev_b32 v162, 16, v128\n\t"
      "v_and_b32 v163, 0xffff0000, v128\n\t"
      "v_pk_add_f32 v[160:161], v[160:161], v[164:165] neg_lo:[0,1] neg_hi:[0,1]\n\t"
      "v_cvt_pk_bf16_f32 v166, v160, v161\n\t"
      "v_lshlrev_b32 v164, 16, v166\n\t"
      "v_and_b32 v165, 0xffff0000, v166\n\t"
      "v_pk_add_f32 v[162:163], v[162:163], v[164:165]\n\t"
      "v_cvt_pk_bf16_f32 v166, v162, v163\n\t"
      "v_cndmask_b32 v166, 0, v166, s[72:73]\n\t"
      "v_lshlrev_b32 v200, 16, v166\n\t"
      "v_and_b32 v201, 0xffff0000, v166\n\t"
      "v_lshlrev_b32 v160, 16, v141\n\t"
      "v_and_b32 v161, 0xffff0000, v141\n\t"
      "v_lshlrev_b32 v162, 16, v145\n\t"
      "v_and_b32 v163, 0xffff0000, v145\n\t"
      "v_lshlrev_b32 v164, 16, v149\n\t"
      "v_and_b32 v165, 0xffff0000, v149\n\t"
      "v_lshlrev_b32 v166, 16, v153\n\t"
      "v_and_b32 v167, 0xffff0000, v153\n\t"
      "v_pk_add_f32 v[160:161], v[160:161], v[162:163]\n\t"
      "v_pk_add_f32 v[164:165], v[164:165], v[166:167]\n\t"
      "v_lshlrev_b32 v162, 16, v129\n\t"
      "v_and_b32 v163, 0xffff0000, v129\n\t"
      "v_pk_add_f32 v[160:161], v[160:161], v[164:165] neg_lo:[0,1] neg_hi:[0,1]\n\t"
      "v_cvt_pk_bf16_f32 v166, v160, v161\n\t"
      "v_lshlrev_b32 v164, 16, v166\n\t"
      "v_and_b32 v165, 0xffff0000, v166\n\t"
      "v_pk_add_f32 v[162:163], v[162:163], v[164:165]\n\t"
      "v_cvt_pk_bf16_f32 v166, v162, v163\n\t"
      "v_cndmask_b32 v166, 0, v166, s[72:73]\n\t"
      "v_lshlrev_b32 v202, 16, v166\n\t"
      "v_and_b32 v203, 0xffff0000, v166\n\t"
      "v_lshlrev_b32 v160, 16, v142\n\t"
      "v_and_b32 v161, 0xffff0000, v142\n\t"
      "v_lshlrev_b32 v162, 16, v146\n\t"
      "v_and_b32 v163, 0xffff0000, v146\n\t"
      "v_lshlrev_b32 v164, 16, v150\n\t"
      "v_and_b32 v165, 0xffff0000, v150\n\t"
      "v_lshlrev_b32 v166, 16, v154\n\t"
      "v_and_b32 v167, 0xffff0000, v154\n\t"
      "v_pk_add_f32 v[160:161], v[160:161], v[162:163]\n\t"
      "v_pk_add_f32 v[164:165], v[164:165], v[166:167]\n\t"
      "v_lshlrev_b32 v162, 16, v130\n\t"
      "v_and_b32 v163, 0xffff0000, v130\n\t"
      "v_pk_add_f32 v[160:161], v[160:161], v[164:165] neg_lo:[0,1] neg_hi:[0,1]\n\t"
      "v_cvt_pk_bf16_f32 v166, v160, v161\n\t"
      "v_lshlrev_b32 v164, 16, v166\n\t"
      "v_and_b32 v165, 0xffff0000, v166\n\t"
      "v_pk_add_f32 v[162:163], v[162:163], v[164:165]\n\t"
      "v_cvt_pk_bf16_f32 v166, v162, v163\n\t"
      "v_cndmask_b32 v166, 0, v166, s[72:73]\n\t"
      "v_lshlrev_b32 v204, 16, v166\n\t"
      "v_and_b32 v205, 0xffff0000, v166\n\t"
      "v_lshlrev_b32 v160, 16, v143\n\t"
      "v_and_b32 v161, 0xffff0000, v143\n\t"
      "v_lshlrev_b32 v162, 16, v147\n\t"
      "v_and_b32 v163, 0xffff0000, v147\n\t"
      "v_lshlrev_b32 v164, 16, v151\n\t"
      "v_and_b32 v165, 0xffff0000, v151\n\t"
      "v_lshlrev_b32 v166, 16, v155\n\t"
      "v_and_b32 v167, 0xffff0000, v155\n\t"
      "v_pk_add_f32 v[160:161], v[160:161], v[162:163]\n\t"
      "v_pk_add_f32 v[164:165], v[164:165], v[166:167]\n\t"
      "v_lshlrev_b32 v162, 16, v131\n\t"
      "v_and_b32 v163, 0xffff0000, v131\n\t"
      "v_pk_add_f32 v[160:161], v[160:161], v[164:165] neg_lo:[0,1] neg_hi:[0,1]\n\t"
      "v_cvt_pk_bf16_f32 v166, v160, v161\n\t"
      "v_lshlrev_b32 v164, 16, v166\n\t"
      "v_and_b32 v165, 0xffff0000, v166\n\t"
      "v_pk_add_f32 v[162:163], v[162:163], v[164:165]\n\t"
      "v_cvt_pk_bf16_f32 v166, v162, v163\n\t"
      "v_cndmask_b32 v166, 0, v166, s[72:73]\n\t"
      "v_lshlrev_b32 v206, 16, v166\n\t"
      "v_and_b32 v207, 0xffff0000, v166\n\t"
      "v_pk_add_f32 v[172:173], v[200:201], v[202:203]\n\t"
      "v_pk_mul_f32 v[174:175], v[200:201], v[200:201]\n\t"
      "v_pk_add_f32 v[160:161], v[204:205], v[206:207]\n\t"
      "v_pk_fma_f32 v[174:175], v[202:203], v[202:203], v[174:175]\n\t"
      "v_pk_add_f32 v[172:173], v[172:173], v[160:161]\n\t"
      "v_pk_fma_f32 v[174:175], v[204:205], v[204:205], v[174:175]\n\t"
      "v_add_f32 v172, v172, v173\n\t"
      "v_pk_fma_f32 v[174:175], v[206:207], v[206:207], v[174:175]\n\t"
      "v_add_f32 v174, v174, v175\n\t"
      "s_nop 1\n\t"
      "v_add_f32_dpp v172, v172, v172 row_ror:1 row_mask:0xf bank_mask:0xf\n\t"
      "v_add_f32_dpp v174, v174, v174 row_ror:1 row_mask:0xf bank_mask:0xf\n\t"
      "s_nop 0\n\t"
      "v_add_f32_dpp v172, v172, v172 row_ror:2 row_mask:0xf bank_mask:0xf\n\t"
      "v_add_f32_dpp v174, v174, v174 row_ror:2 row_mask:0xf bank_mask:0xf\n\t"
      "s_nop 0\n\t"
      "v_add_f32_dpp v172, v172, v172 row_ror:4 row_mask:0xf bank_mask:0xf\n\t"
      "v_add_f32_dpp v174, v174, v174 row_ror:4 row_mask:0xf bank_mask:0xf\n\t"
      "s_nop 0\n\t"
      "v_add_f32_dpp v172, v172, v172 row_ror:8 row_mask:0xf bank_mask:0xf\n\t"
      "v_add_f32_dpp v174, v174, v174 row_ror:8 row_mask:0xf bank_mask:0xf\n\t"
      "s_nop 0\n\t"
      "v_mov_b32 v164, v172\n\t"
      "v_mov_b32 v165, v174\n\t"
      "s_nop 1\n\t"
      "v_permlane16_swap_b32 v172, v164\n\t"
      "v_permlane16_swap_b32 v174, v165\n\t"
      "v_add_f32 v172, v172, v164\n\t"
      "v_add_f32 v174, v174, v165\n\t"
      "v_mov_b32 v164, v172\n\t"
      "v_mov_b32 v165, v174\n\t"
      "s_nop 1\n\t"
      "v_permlane32_swap_b32 v172, v164\n\t"
      "v_permlane32_swap_b32 v174, v165\n\t"
      "v_add_f32 v172, v172, v164\n\t"
      "v_add_f32 v174, v174, v165\n\t"
      "v_mul_f32 v168, 0x3b124925, v172\n\t"
      "v_mul_f32 v170, 0x3b124925, v174\n\t"
      "v_fma_f32 v170, -v168, v168, v170\n\t"
      "v_max_f32 v170, 0, v170\n\t"
      "v_cvt_pk_bf16_f32 v168, v168, v168\n\t"
      "v_lshlrev_b32 v168, 16, v168\n\t"
      "v_cvt_pk_bf16_f32 v170, v170, v170\n\t"
      "v_lshlrev_b32 v170, 16, v170\n\t"
      "v_add_f32 v170, 0x3727c5ac, v170\n\t"
      "v_cvt_pk_bf16_f32 v170, v170, v170\n\t"
      "v_lshlrev_b32 v170, 16, v170\n\t"
      "v_sqrt_f32 v170, v170\n\t"
      "s_nop 0\n\t"
      "v_cvt_pk_bf16_f32 v170, v170, v170\n\t"
      "v_lshlrev_b32 v170, 16, v170\n\t"
      "v_lshrrev_b32 v167, 16, v168\n\t"
      "v_or_b32 v167, v167, v170\n\t"
      "v_rcp_f32 v170, v170\n\t"
      "v_min_u32 v167, 0xfffffffe, v167\n\t"
      "v_mov_b32 v173, v167\n\t"
      "v_pk_add_f32 v[160:161], v[200:201], v[168:169] op_sel_hi:[1,0] neg_lo:[0,1] neg_hi:[0,1]\n\t"
      "v_cvt_pk_bf16_f32 v162, v160, v161\n\t"
      "v_lshlrev_b32 v160, 16, v162\n\t"
      "v_and_b32 v161, 0xffff0000, v162\n\t"
      "v_pk_mul_f32 v[160:161], v[160:161], v[170:171] op_sel_hi:[1,0]\n\t"
      "v_cvt_pk_bf16_f32 v208, v160, v161\n\t"
      "v_pk_add_f32 v[160:161], v[202:203], v[168:169] op_sel_hi:[1,0] neg_lo:[0,1] neg_hi:[0,1]\n\t"
      "v_cvt_pk_bf16_f32 v162, v160, v161\n\t"
      "v_lshlrev_b32 v160, 16, v162\n\t"
      "v_and_b32 v161, 0xffff0000, v162\n\t"
      "v_pk_mul_f32 v[160:161], v[160:161], v[170:171] op_sel_hi:[1,0]\n\t"
      "v_cvt_pk_bf16_f32 v209, v160, v161\n\t"
      "v_pk_add_f32 v[160:161], v[204:205], v[168:169] op_sel_hi:[1,0] neg_lo:[0,1] neg_hi:[0,1]\n\t"
      "v_cvt_pk_bf16_f32 v162, v160, v161\n\t"
      "v_lshlrev_b32 v160, 16, v162\n\t"
      "v_and_b32 v161, 0xffff0000, v162\n\t"
      "v_pk_mul_f32 v[160:161], v[160:161], v[170:171] op_sel_hi:[1,0]\n\t"
      "v_cvt_pk_bf16_f32 v210, v160, v161\n\t"
      "v_pk_add_f32 v[160:161], v[206:207], v[168:169] op_sel_hi:[1,0] neg_lo:[0,1] neg_hi:[0,1]\n\t"
      "v_cvt_pk_bf16_f32 v162, v160, v161\n\t"
      "v_lshlrev_b32 v160, 16, v162\n\t"
      "v_and_b32 v161, 0xffff0000, v162\n\t"
      "v_pk_mul_f32 v[160:161], v[160:161], v[170:171] op_sel_hi:[1,0]\n\t"
      "v_cvt_pk_bf16_f32 v211, v160, v161\n\t"
      "v_readfirstlane_b32 s48, v173\n\t"
      "v_lshlrev_b32 v212, 16, v208\n\t"
      "v_and_b32 v213, 0xffff0000, v208\n\t"
      "v_lshlrev_b32 v160, 16, v132\n\t"
      "v_and_b32 v161, 0xffff0000, v132\n\t"
      "v_pk_mul_f32 v[174:175], v[212:213], v[160:161]\n\t"
      "v_lshlrev_b32 v214, 16, v209\n\t"
      "v_and_b32 v215, 0xffff0000, v209\n\t"
      "v_lshlrev_b32 v160, 16, v133\n\t"
      "v_and_b32 v161, 0xffff0000, v133\n\t"
      "v_pk_fma_f32 v[174:175], v[214:215], v[160:161], v[174:175]\n\t"
      "v_lshlrev_b32 v216, 16, v210\n\t"
      "v_and_b32 v217, 0xffff0000, v210\n\t"
      "v_lshlrev_b32 v160, 16, v134\n\t"
      "v_and_b32 v161, 0xffff0000, v134\n\t"
      "v_pk_fma_f32 v[174:175], v[216:217], v[160:161], v[174:175]\n\t"
      "v_lshlrev_b32 v218, 16, v211\n\t"
      "v_and_b32 v219, 0xffff0000, v211\n\t"
      "v_lshlrev_b32 v160, 16, v135\n\t"
      "v_and_b32 v161, 0xffff0000, v135\n\t"
      "v_pk_fma_f32 v[174:175], v[218:219], v[160:161], v[174:175]\n\t"
      "v_add_f32 v174, v174, v175\n\t"
      "v_cndmask_b32 v174, 0, v174, s[72:73]\n\t"
      "s_nop 1\n\t"
      "v_add_f32_dpp v174, v174, v174 row_ror:1 row_mask:0xf bank_mask:0xf\n\t"
      "s_nop 1\n\t"
      "v_add_f32_dpp v174, v174, v174 row_ror:2 row_mask:0xf bank_mask:0xf\n\t"
      "s_nop 1\n\t"
      "v_add_f32_dpp v174, v174, v174 row_ror:4 row_mask:0xf bank_mask:0xf\n\t"
      "s_nop 1\n\t"
      "v_add_f32_dpp v174, v174, v174 row_ror:8 row_mask:0xf bank_mask:0xf\n\t"
      "s_nop 1\n\t"
      "v_mov_b32 v164, v174\n\t"
      "s_nop 1\n\t"
      "v_permlane16_swap_b32 v174, v164\n\t"
      "v_add_f32 v174, v174, v164\n\t"
      "v_mov_b32 v164, v174\n\t"
      "s_nop 1\n\t"
      "v_permlane32_swap_b32 v174, v164\n\t"
      "v_add_f32 v174, v174, v164\n\t"
      "v_cvt_pk_bf16_f32 v174, v174, v174\n\t"
      "v_pk_add_f32 v[160:161], v[200:201], v[212:213]\n\t"
      "v_cvt_pk_bf16_f32 v220, v160, v161\n\t"
      "v_pk_add_f32 v[160:161], v[202:203], v[214:215]\n\t"
      "v_cvt_pk_bf16_f32 v221, v160, v161\n\t"
      "v_pk_add_f32 v[160:161], v[204:205], v[216:217]\n\t"
      "v_cvt_pk_bf16_f32 v222, v160, v161\n\t"
      "v_pk_add_f32 v[160:161], v[206:207], v[218:219]\n\t"
      "v_cvt_pk_bf16_f32 v223, v160, v161\n\t"
      "v_readfirstlane_b32 s47, v174\n\t"
      "s_nop 0\n\t"
      "s_and_b32 s47, s47, 0xffff\n\t"
      "s_bitcmp1_b32 s47, 15\n\t"
      "s_mov_b32 s52, 0x8000\n\t"
      "s_cselect_b32 s52, 0xffff, s52\n\t"
      "s_xor_b32 s47, s47, s52\n\t"
      "s_lshl_b32 s47, s47, 16\n\t"
      "s_sub_u32 s52, 2047, s46\n\t"
      "s_or_b32 s47, s47, s52\n\t"
      "s_lshl_b32 s52, s40, 2\n\t"
      "s_add_u32 s52, s52, 24576\n\t"
      "v_mov_b32 v160, s52\n\t"
      "v_mov_b32 v161, s47\n\t"
      "v_mov_b32 v162, s48\n\t"
      "s_mov_b64 exec, 1\n\t"
      "ds_write_b32 v160, v161\n\t"
      "ds_write_b32 v160, v162 offset:32\n\t"
      "s_mov_b64 exec, -1\n\t"
      "s_waitcnt lgkmcnt(0)\n\t"
      "s_barrier\n\t"
      "s_cmp_eq_u32 s40, 0\n\t"
      "s_cbranch_scc0 L_nokey_%=\n\t"
      "v_add_u32 v160, 24576, v107\n\t"
      "s_mov_b64 exec, 0xf\n\t"
      "ds_read_b128 v[164:167], v160\n\t"
      "s_lshl_b32 s52, s43, 7\n\t"
      "s_add_u32 s52, s52, 65536\n\t"
      "s_waitcnt lgkmcnt(0)\n\t"
      "buffer_store_dwordx4 v[164:167], v107, s[64:67], s52 offen sc1\n\t"
      "s_mov_b64 exec, -1\n\t"
      "L_nokey_%=:\n\t"
      "s_mul_i32 s52, s46, 896\n\t"
      "v_mov_b32 v160, 0x7ffffff0\n\t"
      "v_cndmask_b32 v160, v160, v107, s[72:73]\n\t"
      "buffer_store_dwordx4 v[220:223], v160, s[24:27], s52 offen sc1\n\t"
      "s_and_b32 s52, s10, s11\n\t"
      "s_cmp_lt_i32 s52, 0\n\t"
      "s_cbranch_scc0 L_moe_%=\n\t"
      "s_waitcnt vmcnt(0)\n\t"
      "s_lshl_b32 s52, s43, 7\n\t"
      "s_lshl_b32 s53, s40, 2\n\t"
      "s_add_u32 s52, s52, s53\n\t"
      "s_add_u32 s52, s52, 131072\n\t"
      "v_mov_b32 v160, 0\n\t"
      "s_mov_b64 exec, 1\n\t"
      "buffer_store_dword v160, v160, s[64:67], s52 offen sc1\n\t"
      "s_mov_b64 exec, -1\n\t"
      "s_branch L_end_%=\n\t"
      "L_moe_%=:\n\t"
      "s_cmp_lt_u32 s40, 4\n\t"
      "s_cbranch_scc0 L_f0allx_%=\n\t"
      "s_mov_b32 s14, 0\n\t"
      "L_f0all_%=:\n\t"
      "v_cmp_ne_u32 vcc, s44, v136\n\t"
      "v_cmp_ne_u32 s[68:69], s45, v137\n\t"
      "s_nop 1\n\t"
      "s_or_b64 vcc, vcc, s[68:69]\n\t"
      "s_cbranch_scc0 L_f0allx_%=\n\t"
      "s_add_u32 s14, s14, 1\n\t"
      "s_cmp_lt_u32 s14, 0x10000\n\t"
      "s_cbranch_scc0 L_f0allx_%=\n\t"
      "s_mov_b32 s52, 40960\n\t"
      "s_lshl_b32 s53, s40, 9\n\t"
      "s_add_u32 s52, s52, s53\n\t"
      "v_lshlrev_b32 v160, 3, v104\n\t"
      "buffer_load_dwordx2 v[136:137], v160, s[64:67], s52 offen sc1\n\t"
      "s_waitcnt vmcnt(0)\n\t"
      "s_branch L_f0all_%=\n\t"
      "L_f0allx_%=:\n\t"
      "s_barrier\n\t"
      "v_lshrrev_b32 v144, 1, v104\n\t"
      "v_lshlrev_b32 v144, 7, v144\n\t"
      "v_and_b32 v160, 1, v104\n\t"
      "v_lshl_add_u32 v144, v160, 4, v144\n\t"
      "s_lshl_b32 s54, s40, 12\n\t"
      "s_add_u32 s54, s54, 65536\n\t"
      "buffer_load_dwordx4 v[140:143], v144, s[64:67], s54 offen sc1\n\t"
      "v_lshrrev_b32 v160, 4, v104\n\t"
      "v_lshlrev_b32 v160, 3, v160\n\t"
      "v_lshrrev_b32 v161, 2, v105\n\t"
      "v_add_u32 v160, v160, v161\n\t"
      "v_lshlrev_b32 v160, 5, v160\n\t"
      "v_and_b32 v161, 3, v104\n\t"
      "v_lshl_add_u32 v245, v161, 3, v160\n\t"
      "s_cmp_lt_i32 s10, 0\n\t"
      "s_cbranch_scc1 L_nogufr_%=\n\t"
      "s_min_u32 s52, s40, 6\n\t"
      "s_lshl_b32 s52, s52, 11\n\t"
      "s_add_u32 s52, s52, 131072\n\t"
      "v_add_u32 v160, s52, v245\n\t"
      "ds_read_b64_tr_b16 v[176:177], v160 offset:0\n\t"
      "ds_read_b64_tr_b16 v[184:185], v160 offset:14336\n\t"
      "ds_read_b64_tr_b16 v[178:179], v160 offset:128\n\t"
      "ds_read_b64_tr_b16 v[186:187], v160 offset:14464\n\t"
      "ds_read_b64_tr_b16 v[180:181], v160 offset:1024\n\t"
      "ds_read_b64_tr_b16 v[188:189], v160 offset:15360\n\t"
      "ds_read_b64_tr_b16 v[182:183], v160 offset:1152\n\t"
      "ds_read_b64_tr_b16 v[190:191], v160 offset:15488\n\t"
      "L_nogufr_%=:\n\t"
      "s_waitcnt vmcnt(0)\n\t"
      "s_lshl_b32 s52, s43, 7\n\t"
      "s_lshl_b32 s53, s40, 2\n\t"
      "s_add_u32 s52, s52, s53\n\t"
      "s_add_u32 s52, s52, 131072\n\t"
      "v_mov_b32 v160, 0\n\t"
      "s_mov_b64 exec, 1\n\t"
      "buffer_store_dword v160, v160, s[64:67], s52 offen sc1\n\t"
      "s_mov_b64 exec, -1\n\t"
      "s_mov_b32 s14, 0\n\t"
      "L_kp_%=:\n\t"
      "v_max3_u32 v160, v140, v141, v142\n\t"
      "v_max_u32 v160, v143, v160\n\t"
      "v_cmp_eq_u32 vcc, -1, v160\n\t"
      "s_cbranch_vccz L_kpx_%=\n\t"
      "s_add_u32 s14, s14, 1\n\t"
      "s_cmp_lt_u32 s14, 0x10000\n\t"
      "s_cbranch_scc0 L_kpx_%=\n\t"
      "s_mov_b64 exec, vcc\n\t"
      "buffer_load_dwordx4 v[140:143], v144, s[64:67], s54 offen sc1\n\t"
      "s_mov_b64 exec, -1\n\t"
      "s_waitcnt vmcnt(0)\n\t"
      "s_branch L_kp_%=\n\t"
      "L_kpx_%=:\n\t"
      "v_max3_u32 v160, v140, v141, v142\n\t"
      "v_max_u32 v160, v160, v143\n\t"
      "s_nop 1\n\t"
      "v_max_u32_dpp v160, v160, v160 row_ror:1 row_mask:0xf bank_mask:0xf\n\t"
      "s_nop 1\n\t"
      "v_max_u32_dpp v160, v160, v160 row_ror:2 row_mask:0xf bank_mask:0xf\n\t"
      "s_nop 1\n\t"
      "v_max_u32_dpp v160, v160, v160 row_ror:4 row_mask:0xf bank_mask:0xf\n\t"
      "s_nop 1\n\t"
      "v_max_u32_dpp v160, v160, v160 row_ror:8 row_mask:0xf bank_mask:0xf\n\t"
      "s_nop 1\n\t"
      "v_mov_b32 v164, v160\n\t"
      "s_nop 1\n\t"
      "v_permlane16_swap_b32 v160, v164\n\t"
      "v_max_u32 v160, v160, v164\n\t"
      "v_mov_b32 v164, v160\n\t"
      "s_nop 1\n\t"
      "v_permlane32_swap_b32 v160, v164\n\t"
      "v_max_u32 v160, v160, v164\n\t"
      "s_lshl_b32 s52, s40, 2\n\t"
      "v_mov_b32 v161, s52\n\t"
      "s_mov_b64 exec, 1\n\t"
      "ds_write_b32 v161, v160 offset:0\n\t"
      "s_mov_b64 exec, -1\n\t"
      "s_cmp_eq_u32 s40, 7\n\t"
      "s_cbranch_scc0 L_nozero_%=\n\t"
      "v_mov_b32 v162, 0\n\t"
      "v_lshlrev_b32 v161, 2, v104\n\t"
      "ds_write_b32 v161, v162 offset:32\n\t"
      "L_nozero_%=:\n\t"
      "s_waitcnt lgkmcnt(0)\n\t"
      "s_barrier\n\t"
      "v_mov_b32 v160, 0\n\t"
      "ds_read_b128 v[164:167], v160 offset:0\n\t"
      "ds_read_b128 v[146:149], v160 offset:16\n\t"
      "s_waitcnt lgkmcnt(0)\n\t"
      "v_min3_u32 v164, v164, v165, v166\n\t"
      "v_min3_u32 v164, v164, v167, v146\n\t"
      "v_min3_u32 v164, v164, v147, v148\n\t"
      "v_min_u32 v164, v164, v149\n\t"
      "v_cmp_ge_u32 s[68:69], v140, v164\n\t"
      "v_cmp_ge_u32 s[70:71], v141, v164\n\t"
      "v_cmp_ge_u32 s[60:61], v142, v164\n\t"
      "v_cmp_ge_u32 s[62:63], v143, v164\n\t"
      "s_nop 0\n\t"
      "s_bcnt1_i32_b64 s52, s[68:69]\n\t"
      "s_bcnt1_i32_b64 s53, s[70:71]\n\t"
      "s_bcnt1_i32_b64 s54, s[60:61]\n\t"
      "s_bcnt1_i32_b64 s55, s[62:63]\n\t"
      "s_add_u32 s49, s52, s53\n\t"
      "s_add_u32 s49, s49, s54\n\t"
      "s_add_u32 s49, s49, s55\n\t"
      "v_mov_b32 v161, s49\n\t"
      "s_mov_b64 exec, 1\n\t"
      "ds_add_rtn_u32 v162, v160, v161 offset:32\n\t"
      "s_mov_b64 exec, -1\n\t"
      "s_waitcnt lgkmcnt(0)\n\t"
      "v_readfirstlane_b32 s51, v162\n\t"
      "s_nop 0\n\t"
      "s_lshl_b32 s51, s51, 2\n\t"
      "v_mbcnt_lo_u32_b32 v161, s68, 0\n\t"
      "v_mbcnt_hi_u32_b32 v161, s69, v161\n\t"
      "v_lshlrev_b32 v161, 2, v161\n\t"
      "v_add_u32 v161, s51, v161\n\t"
      "s_mov_b64 exec, s[68:69]\n\t"
      "ds_write_b32 v161, v140 offset:64\n\t"
      "s_mov_b64 exec, -1\n\t"
      "s_lshl_b32 s52, s52, 2\n\t"
      "s_add_u32 s51, s51, s52\n\t"
      "v_mbcnt_lo_u32_b32 v161, s70, 0\n\t"
      "v_mbcnt_hi_u32_b32 v161, s71, v161\n\t"
      "v_lshlrev_b32 v161, 2, v161\n\t"
      "v_add_u32 v161, s51, v161\n\t"
      "s_mov_b64 exec, s[70:71]\n\t"
      "ds_write_b32 v161, v141 offset:64\n\t"
      "s_mov_b64 exec, -1\n\t"
      "s_lshl_b32 s53, s53, 2\n\t"
      "s_add_u32 s51, s51, s53\n\t"
      "v_mbcnt_lo_u32_b32 v161, s60, 0\n\t"
      "v_mbcnt_hi_u32_b32 v161, s61, v161\n\t"
      "v_lshlrev_b32 v161, 2, v161\n\t"
      "v_add_u32 v161, s51, v161\n\t"
      "s_mov_b64 exec, s[60:61]\n\t"
      "ds_write_b32 v161, v142 offset:64\n\t"
      "s_mov_b64 exec, -1\n\t"
      "s_lshl_b32 s54, s54, 2\n\t"
      "s_add_u32 s51, s51, s54\n\t"
      "v_mbcnt_lo_u32_b32 v161, s62, 0\n\t"
      "v_mbcnt_hi_u32_b32 v161, s63, v161\n\t"
      "v_lshlrev_b32 v161, 2, v161\n\t"
      "v_add_u32 v161, s51, v161\n\t"
      "s_mov_b64 exec, s[62:63]\n\t"
      "ds_write_b32 v161, v143 offset:64\n\t"
      "s_mov_b64 exec, -1\n\t"
      "s_waitcnt lgkmcnt(0)\n\t"
      "s_barrier\n\t"
      "ds_read_b32 v161, v160 offset:32\n\t"
      "v_and_b32 v162, 31, v104\n\t"
      "v_lshlrev_b32 v162, 2, v162\n\t"
      "ds_read_b32 v163, v162 offset:64\n\t"
      "v_lshrrev_b32 v162, 5, v104\n\t"
      "v_lshlrev_b32 v162, 6, v162\n\t"
      "ds_read_b128 v[224:227], v162 offset:64\n\t"
      "ds_read_b128 v[228:231], v162 offset:80\n\t"
      "ds_read_b128 v[232:235], v162 offset:96\n\t"
      "ds_read_b128 v[236:239], v162 offset:112\n\t"
      "s_waitcnt lgkmcnt(5)\n\t"
      "v_readfirstlane_b32 s49, v161\n\t"
      "s_lshl_b32 s50, s40, 5\n\t"
      "s_add_u32 s50, s50, 8448\n\t"
      "s_cmp_le_u32 s49, 32\n\t"
      "s_cbranch_scc0 L_slow8_%=\n\t"
      "s_waitcnt lgkmcnt(0)\n\t"
      "v_mov_b32 v164, 0\n\t"
      "v_cmp_gt_u32 s[68:69], v224, v163\n\t"
      "v_cmp_gt_u32 s[70:71], v225, v163\n\t"
      "v_cmp_gt_u32 s[60:61], v226, v163\n\t"
      "v_cmp_gt_u32 s[62:63], v227, v163\n\t"
      "v_addc_co_u32 v164, vcc, 0, v164, s[68:69]\n\t"
      "v_addc_co_u32 v164, vcc, 0, v164, s[70:71]\n\t"
      "v_addc_co_u32 v164, vcc, 0, v164, s[60:61]\n\t"
      "v_addc_co_u32 v164, vcc, 0, v164, s[62:63]\n\t"
      "v_cmp_gt_u32 s[68:69], v228, v163\n\t"
      "v_cmp_gt_u32 s[70:71], v229, v163\n\t"
      "v_cmp_gt_u32 s[60:61], v230, v163\n\t"
      "v_cmp_gt_u32 s[62:63], v231, v163\n\t"
      "v_addc_co_u32 v164, vcc, 0, v164, s[68:69]\n\t"
      "v_addc_co_u32 v164, vcc, 0, v164, s[70:71]\n\t"
      "v_addc_co_u32 v164, vcc, 0, v164, s[60:61]\n\t"
      "v_addc_co_u32 v164, vcc, 0, v164, s[62:63]\n\t"
      "v_cmp_gt_u32 s[68:69], v232, v163\n\t"
      "v_cmp_gt_u32 s[70:71], v233, v163\n\t"
      "v_cmp_gt_u32 s[60:61], v234, v163\n\t"
      "v_cmp_gt_u32 s[62:63], v235, v163\n\t"
      "v_addc_co_u32 v164, vcc, 0, v164, s[68:69]\n\t"
      "v_addc_co_u32 v164, vcc, 0, v164, s[70:71]\n\t"
      "v_addc_co_u32 v164, vcc, 0, v164, s[60:61]\n\t"
      "v_addc_co_u32 v164, vcc, 0, v164, s[62:63]\n\t"
      "v_cmp_gt_u32 s[68:69], v236, v163\n\t"
      "v_cmp_gt_u32 s[70:71], v237, v163\n\t"
      "v_cmp_gt_u32 s[60:61], v238, v163\n\t"
      "v_cmp_gt_u32 s[62:63], v239, v163\n\t"
      "v_addc_co_u32 v164, vcc, 0, v164, s[68:69]\n\t"
      "v_addc_co_u32 v164, vcc, 0, v164, s[70:71]\n\t"
      "v_addc_co_u32 v164, vcc, 0, v164, s[60:61]\n\t"
      "v_addc_co_u32 v164, vcc, 0, v164, s[62:63]\n\t"
      "v_mov_b32 v165, v164\n\t"
      "s_nop 1\n\t"
      "v_permlane32_swap_b32 v164, v165\n\t"
      "v_add_u32 v164, v164, v165\n\t"
      "v_cmp_gt_u32 vcc, s49, v104\n\t"
      "v_cmp_gt_u32 s[68:69], 8, v164\n\t"
      "v_lshlrev_b32 v164, 2, v164\n\t"
      "s_and_b64 vcc, vcc, s[68:69]\n\t"
      "v_add_u32 v164, s50, v164\n\t"
      "s_mov_b64 exec, vcc\n\t"
      "ds_write_b32 v164, v163\n\t"
      "s_mov_b64 exec, -1\n\t"
      "s_branch L_have8_%=\n\t"
      "L_slow8_%=:\n\t"
      "s_waitcnt lgkmcnt(0)\n\t"
      "s_mov_b32 s52, 0\n\t"
      "L_slowc_%=:\n\t"
      "v_add_u32 v161, s52, v104\n\t"
      "v_cmp_gt_u32 s[70:71], s49, v161\n\t"
      "v_lshlrev_b32 v162, 2, v161\n\t"
      "ds_read_b32 v163, v162 offset:64\n\t"
      "v_mov_b32 v164, 0\n\t"
      "s_mov_b32 s53, 0\n\t"
      "L_slowj_%=:\n\t"
      "s_lshl_b32 s54, s53, 2\n\t"
      "v_mov_b32 v165, s54\n\t"
      "ds_read_b32 v166, v165 offset:64\n\t"
      "s_waitcnt lgkmcnt(0)\n\t"
      "v_cmp_gt_u32 vcc, v166, v163\n\t"
      "s_nop 1\n\t"
      "v_addc_co_u32 v164, vcc, 0, v164, vcc\n\t"
      "s_add_u32 s53, s53, 1\n\t"
      "s_cmp_lt_u32 s53, s49\n\t"
      "s_cbranch_scc1 L_slowj_%=\n\t"
      "v_cmp_gt_u32 vcc, 8, v164\n\t"
      "v_lshlrev_b32 v164, 2, v164\n\t"
      "s_and_b64 vcc, vcc, s[70:71]\n\t"
      "v_add_u32 v164, s50, v164\n\t"
      "s_mov_b64 exec, vcc\n\t"
      "ds_write_b32 v164, v163\n\t"
      "s_mov_b64 exec, -1\n\t"
      "s_add_u32 s52, s52, 64\n\t"
      "s_cmp_lt_u32 s52, s49\n\t"
      "s_cbranch_scc1 L_slowc_%=\n\t"
      "L_have8_%=:\n\t"
      "s_waitcnt lgkmcnt(0)\n\t"
      "s_or_b32 s52, s43, s40\n\t"
      "s_cmp_eq_u32 s52, 0\n\t"
      "s_cbranch_scc0 L_nogen_%=\n\t"
      "v_mov_b32 v160, s44\n\t"
      "v_mov_b32 v161, s45\n\t"
      "v_mov_b32 v162, 0\n\t"
      "s_mov_b64 exec, 1\n\t"
      "buffer_store_dwordx2 v[160:161], v162, s[64:67], 0 offen\n\t"
      "s_mov_b64 exec, -1\n\t"
      "L_nogen_%=:\n\t"
      "s_cmp_lt_i32 s10, 0\n\t"
      "s_cbranch_scc1 L_down_%=\n\t"
      "s_cmp_lt_u32 s40, 7\n\t"
      "s_cbranch_scc0 L_gured_%=\n\t"
      "v_and_b32 v160, 7, v104\n\t"
      "v_lshlrev_b32 v160, 2, v160\n\t"
      "v_add_u32 v160, s50, v160\n\t"
      "ds_read_b32 v150, v160\n\t"
      "v_lshrrev_b32 v151, 3, v105\n\t"
      "s_lshl_b32 s52, s40, 1\n\t"
      "v_add_u32 v151, s52, v151\n\t"
      "v_lshl_add_u32 v151, v151, 2, v106\n\t"
      "v_mul_u32_u24 v161, 4682, v151\n\t"
      "v_lshrrev_b32 v161, 16, v161\n\t"
      "v_lshlrev_b32 v151, 4, v151\n\t"
      "v_lshl_add_u32 v161, v161, 5, v151\n\t"
      "s_waitcnt lgkmcnt(0)\n\t"
      "v_and_b32 v150, 0x7ff, v150\n\t"
      "v_sub_u32 v150, 2047, v150\n\t"
      "v_lshl_add_u32 v161, v150, 10, v161\n\t"
      "v_mul_u32_u24 v162, 896, v150\n\t"
      "v_add_u32 v162, v162, v151\n\t"
      "v_lshrrev_b32 v163, 3, v150\n\t"
      "v_lshlrev_b32 v163, 7, v163\n\t"
      "v_and_b32 v164, 7, v150\n\t"
      "v_lshl_add_u32 v163, v164, 2, v163\n\t"
      "s_mov_b32 s52, 5242880\n\t"
      "buffer_load_dwordx4 v[140:143], v161, s[64:67], s52 offen sc1\n\t"
      "s_mov_b32 s52, 7340032\n\t"
      "buffer_load_dwordx4 v[144:147], v161, s[64:67], s52 offen sc1\n\t"
      "s_mov_b32 s52, 9437184\n\t"
      "buffer_load_dwordx4 v[148:151], v161, s[64:67], s52 offen sc1\n\t"
      "s_mov_b32 s52, 11534336\n\t"
      "buffer_load_dwordx4 v[152:155], v161, s[64:67], s52 offen sc1\n\t"
      "buffer_load_dwordx4 v[128:131], v162, s[20:23], 0 offen\n\t"
      "s_mov_b32 s52, 65568\n\t"
      "buffer_load_dword v136, v163, s[64:67], s52 offen sc1\n\t"
      "s_waitcnt vmcnt(0)\n\t"
      "v_lshlrev_b32 v168, 16, v136\n\t"
      "v_and_b32 v170, 0xffff0000, v136\n\t"
      "v_rcp_f32 v170, v170\n\t"
      "v_lshlrev_b32 v160, 16, v140\n\t"
      "v_and_b32 v161, 0xffff0000, v140\n\t"
      "v_lshlrev_b32 v162, 16, v144\n\t"
      "v_and_b32 v163, 0xffff0000, v144\n\t"
      "v_lshlrev_b32 v164, 16, v148\n\t"
      "v_and_b32 v165, 0xffff0000, v148\n\t"
      "v_lshlrev_b32 v166, 16, v152\n\t"
      "v_and_b32 v167, 0xffff0000, v152\n\t"
      "v_pk_add_f32 v[160:161], v[160:161], v[162:163]\n\t"
      "v_pk_add_f32 v[164:165], v[164:165], v[166:167]\n\t"
      "v_lshlrev_b32 v162, 16, v128\n\t"
      "v_and_b32 v163, 0xffff0000, v128\n\t"
      "v_pk_add_f32 v[160:161], v[160:161], v[164:165] neg_lo:[0,1] neg_hi:[0,1]\n\t"
      "v_cvt_pk_bf16_f32 v166, v160, v161\n\t"
      "v_lshlrev_b32 v164, 16, v166\n\t"
      "v_and_b32 v165, 0xffff0000, v166\n\t"
      "v_pk_add_f32 v[162:163], v[162:163], v[164:165]\n\t"
      "v_cvt_pk_bf16_f32 v166, v162, v163\n\t"
      "v_lshlrev_b32 v200, 16, v166\n\t"
      "v_and_b32 v201, 0xffff0000, v166\n\t"
      "v_lshlrev_b32 v160, 16, v141\n\t"
      "v_and_b32 v161, 0xffff0000, v141\n\t"
      "v_lshlrev_b32 v162, 16, v145\n\t"
      "v_and_b32 v163, 0xffff0000, v145\n\t"
      "v_lshlrev_b32 v164, 16, v149\n\t"
      "v_and_b32 v165, 0xffff0000, v149\n\t"
      "v_lshlrev_b32 v166, 16, v153\n\t"
      "v_and_b32 v167, 0xffff0000, v153\n\t"
      "v_pk_add_f32 v[160:161], v[160:161], v[162:163]\n\t"
      "v_pk_add_f32 v[164:165], v[164:165], v[166:167]\n\t"
      "v_lshlrev_b32 v162, 16, v129\n\t"
      "v_and_b32 v163, 0xffff0000, v129\n\t"
      "v_pk_add_f32 v[160:161], v[160:161], v[164:165] neg_lo:[0,1] neg_hi:[0,1]\n\t"
      "v_cvt_pk_bf16_f32 v166, v160, v161\n\t"
      "v_lshlrev_b32 v164, 16, v166\n\t"
      "v_and_b32 v165, 0xffff0000, v166\n\t"
      "v_pk_add_f32 v[162:163], v[162:163], v[164:165]\n\t"
      "v_cvt_pk_bf16_f32 v166, v162, v163\n\t"
      "v_lshlrev_b32 v202, 16, v166\n\t"
      "v_and_b32 v203, 0xffff0000, v166\n\t"
      "v_lshlrev_b32 v160, 16, v142\n\t"
      "v_and_b32 v161, 0xffff0000, v142\n\t"
      "v_lshlrev_b32 v162, 16, v146\n\t"
      "v_and_b32 v163, 0xffff0000, v146\n\t"
      "v_lshlrev_b32 v164, 16, v150\n\t"
      "v_and_b32 v165, 0xffff0000, v150\n\t"
      "v_lshlrev_b32 v166, 16, v154\n\t"
      "v_and_b32 v167, 0xffff0000, v154\n\t"
      "v_pk_add_f32 v[160:161], v[160:161], v[162:163]\n\t"
      "v_pk_add_f32 v[164:165], v[164:165], v[166:167]\n\t"
      "v_lshlrev_b32 v162, 16, v130\n\t"
      "v_and_b32 v163, 0xffff0000, v130\n\t"
      "v_pk_add_f32 v[160:161], v[160:161], v[164:165] neg_lo:[0,1] neg_hi:[0,1]\n\t"
      "v_cvt_pk_bf16_f32 v166, v160, v161\n\t"
      "v_lshlrev_b32 v164, 16, v166\n\t"
      "v_and_b32 v165, 0xffff0000, v166\n\t"
      "v_pk_add_f32 v[162:163], v[162:163], v[164:165]\n\t"
      "v_cvt_pk_bf16_f32 v166, v162, v163\n\t"
      "v_lshlrev_b32 v204, 16, v166\n\t"
      "v_and_b32 v205, 0xffff0000, v166\n\t"
      "v_lshlrev_b32 v160, 16, v143\n\t"
      "v_and_b32 v161, 0xffff0000, v143\n\t"
      "v_lshlrev_b32 v162, 16, v147\n\t"
      "v_and_b32 v163, 0xffff0000, v147\n\t"
      "v_lshlrev_b32 v164, 16, v151\n\t"
      "v_and_b32 v165, 0xffff0000, v151\n\t"
      "v_lshlrev_b32 v166, 16, v155\n\t"
      "v_and_b32 v167, 0xffff0000, v155\n\t"
      "v_pk_add_f32 v[160:161], v[160:161], v[162:163]\n\t"
      "v_pk_add_f32 v[164:165], v[164:165], v[166:167]\n\t"
      "v_lshlrev_b32 v162, 16, v131\n\t"
      "v_and_b32 v163, 0xffff0000, v131\n\t"
      "v_pk_add_f32 v[160:161], v[160:161], v[164:165] neg_lo:[0,1] neg_hi:[0,1]\n\t"
      "v_cvt_pk_bf16_f32 v166, v160, v161\n\t"
      "v_lshlrev_b32 v164, 16, v166\n\t"
      "v_and_b32 v165, 0xffff0000, v166\n\t"
      "v_pk_add_f32 v[162:163], v[162:163], v[164:165]\n\t"
      "v_cvt_pk_bf16_f32 v166, v162, v163\n\t"
      "v_lshlrev_b32 v206, 16, v166\n\t"
      "v_and_b32 v207, 0xffff0000, v166\n\t"
      "v_pk_add_f32 v[160:161], v[200:201], v[168:169] op_sel_hi:[1,0] neg_lo:[0,1] neg_hi:[0,1]\n\t"
      "v_cvt_pk_bf16_f32 v162, v160, v161\n\t"
      "v_lshlrev_b32 v160, 16, v162\n\t"
      "v_and_b32 v161, 0xffff0000, v162\n\t"
      "v_pk_mul_f32 v[160:161], v[160:161], v[170:171] op_sel_hi:[1,0]\n\t"
      "v_cvt_pk_bf16_f32 v208, v160, v161\n\t"
      "v_pk_add_f32 v[160:161], v[202:203], v[168:169] op_sel_hi:[1,0] neg_lo:[0,1] neg_hi:[0,1]\n\t"
      "v_cvt_pk_bf16_f32 v162, v160, v161\n\t"
      "v_lshlrev_b32 v160, 16, v162\n\t"
      "v_and_b32 v161, 0xffff0000, v162\n\t"
      "v_pk_mul_f32 v[160:161], v[160:161], v[170:171] op_sel_hi:[1,0]\n\t"
      "v_cvt_pk_bf16_f32 v209, v160, v161\n\t"
      "v_pk_add_f32 v[160:161], v[204:205], v[168:169] op_sel_hi:[1,0] neg_lo:[0,1] neg_hi:[0,1]\n\t"
      "v_cvt_pk_bf16_f32 v162, v160, v161\n\t"
      "v_lshlrev_b32 v160, 16, v162\n\t"
      "v_and_b32 v161, 0xffff0000, v162\n\t"
      "v_pk_mul_f32 v[160:161], v[160:161], v[170:171] op_sel_hi:[1,0]\n\t"
      "v_cvt_pk_bf16_f32 v210, v160, v161\n\t"
      "v_pk_add_f32 v[160:161], v[206:207], v[168:169] op_sel_hi:[1,0] neg_lo:[0,1] neg_hi:[0,1]\n\t"
      "v_cvt_pk_bf16_f32 v162, v160, v161\n\t"
      "v_lshlrev_b32 v160, 16, v162\n\t"
      "v_and_b32 v161, 0xffff0000, v162\n\t"
      "v_pk_mul_f32 v[160:161], v[160:161], v[170:171] op_sel_hi:[1,0]\n\t"
      "v_cvt_pk_bf16_f32 v211, v160, v161\n\t"
      "s_nop 1\n\t"
      "v_mov_b32_dpp v212, v208 row_ror:8 row_mask:0xf bank_mask:0xf\n\t"
      "v_mov_b32_dpp v213, v209 row_ror:8 row_mask:0xf bank_mask:0xf\n\t"
      "v_mov_b32_dpp v214, v210 row_ror:8 row_mask:0xf bank_mask:0xf\n\t"
      "v_mov_b32_dpp v215, v211 row_ror:8 row_mask:0xf bank_mask:0xf\n\t"
      "s_waitcnt lgkmcnt(0)\n\t"
      "s_nop 0\n\t"
      "v_mfma_f32_16x16x32_bf16 v[192:195], v[208:211], v[176:179], 0\n\t"
      "v_mfma_f32_16x16x32_bf16 v[196:199], v[208:211], v[184:187], 0\n\t"
      "v_mfma_f32_16x16x32_bf16 v[192:195], v[212:215], v[180:183], v[192:195]\n\t"
      "v_mfma_f32_16x16x32_bf16 v[196:199], v[212:215], v[188:191], v[196:199]\n\t"
      "s_lshl_b32 s52, s40, 11\n\t"
      "s_add_u32 s52, s52, 8704\n\t"
      "v_add_u32 v160, s52, v107\n\t"
      "s_nop 5\n\t"
      "ds_write_b128 v160, v[192:195]\n\t"
      "ds_write_b128 v160, v[196:199] offset:1024\n\t"
      "s_waitcnt lgkmcnt(0)\n\t"
      "L_gured_%=:\n\t"
      "s_barrier\n\t"
      "s_cmp_lt_u32 s40, 2\n\t"
      "s_cbranch_scc0 L_end19_%=\n\t"
      "s_lshl_b32 s52, s40, 2\n\t"
      "v_add_u32 v159, s52, v106\n\t"
      "v_lshrrev_b32 v160, 2, v159\n\t"
      "v_lshl_add_u32 v160, v160, 4, v105\n\t"
      "v_lshlrev_b32 v160, 4, v160\n\t"
      "v_and_b32 v161, 3, v159\n\t"
      "v_lshl_add_u32 v160, v161, 2, v160\n\t"
      "ds_read_b32 v140, v160 offset:8704\n\t"
      "ds_read_b32 v148, v160 offset:9728\n\t"
      "ds_read_b32 v141, v160 offset:10752\n\t"
      "ds_read_b32 v149, v160 offset:11776\n\t"
      "ds_read_b32 v142, v160 offset:12800\n\t"
      "ds_read_b32 v150, v160 offset:13824\n\t"
      "ds_read_b32 v143, v160 offset:14848\n\t"
      "ds_read_b32 v151, v160 offset:15872\n\t"
      "ds_read_b32 v144, v160 offset:16896\n\t"
      "ds_read_b32 v152, v160 offset:17920\n\t"
      "ds_read_b32 v145, v160 offset:18944\n\t"
      "ds_read_b32 v153, v160 offset:19968\n\t"
      "ds_read_b32 v146, v160 offset:20992\n\t"
      "ds_read_b32 v154, v160 offset:22016\n\t"
      "s_waitcnt lgkmcnt(0)\n\t"
      "v_add_f32 v140, v140, v141\n\t"
      "v_add_f32 v142, v142, v143\n\t"
      "v_add_f32 v144, v144, v145\n\t"
      "v_add_f32 v140, v140, v142\n\t"
      "v_add_f32 v144, v144, v146\n\t"
      "v_add_f32 v140, v140, v144\n\t"
      "v_add_f32 v148, v148, v149\n\t"
      "v_add_f32 v150, v150, v151\n\t"
      "v_add_f32 v152, v152, v153\n\t"
      "v_add_f32 v148, v148, v150\n\t"
      "v_add_f32 v152, v152, v154\n\t"
      "v_add_f32 v148, v148, v152\n\t"
      "v_cvt_pk_bf16_f32 v140, v140, v140\n\t"
      "v_lshlrev_b32 v140, 16, v140\n\t"
      "v_cvt_pk_bf16_f32 v148, v148, v148\n\t"
      "v_lshlrev_b32 v148, 16, v148\n\t"
      "v_mul_f32 v160, 0xbfb8aa3b, v140\n\t"
      "v_exp_f32 v160, v160\n\t"
      "s_nop 0\n\t"
      "v_add_f32 v160, 1.0, v160\n\t"
      "v_rcp_f32 v160, v160\n\t"
      "s_nop 0\n\t"
      "v_mul_f32 v160, v140, v160\n\t"
      "v_cvt_pk_bf16_f32 v160, v160, v160\n\t"
      "v_lshlrev_b32 v160, 16, v160\n\t"
      "v_mul_f32 v160, v160, v148\n\t"
      "s_nop 1\n\t"
      "v_mov_b32_dpp v161, v160 quad_perm:[1,0,3,2] row_mask:0xf bank_mask:0xf\n\t"
      "v_cvt_pk_bf16_f32 v164, v160, v161\n\t"
      "v_min_u32 v164, 0xfffffffe, v164\n\t"
      "s_nop 1\n\t"
      "v_mov_b32_dpp v165, v164 quad_perm:[2,3,0,1] row_mask:0xf bank_mask:0xf\n\t"
      "v_mov_b32_dpp v166, v164 row_shl:4 row_mask:0xf bank_mask:0xf bound_ctrl:1\n\t"
      "s_nop 0\n\t"
      "v_mov_b32_dpp v167, v165 row_shl:4 row_mask:0xf bank_mask:0xf bound_ctrl:1\n\t"
      "v_lshlrev_b32 v160, 5, v159\n\t"
      "v_lshl_add_u32 v160, v105, 1, v160\n\t"
      "v_and_b32 v161, 7, v105\n\t"
      "v_cmp_eq_u32 vcc, 0, v161\n\t"
      "s_lshl_b32 s52, s10, 3\n\t"
      "s_add_u32 s52, s52, 98304\n\t"
      "s_mov_b64 exec, vcc\n\t"
      "buffer_store_dwordx4 v[164:167], v160, s[64:67], s52 offen sc1\n\t"
      "s_mov_b64 exec, -1\n\t"
      "L_end19_%=:\n\t"
      "s_branch L_end_%=\n\t"
      "L_down_%=:\n\t"
      "v_lshrrev_b32 v160, 1, v104\n\t"
      "v_mul_u32_u24 v160, 896, v160\n\t"
      "v_and_b32 v161, 1, v104\n\t"
      "v_lshl_add_u32 v160, v161, 4, v160\n\t"
      "v_add_u32 v160, s11, v160\n\t"
      "s_mul_i32 s52, s40, 7\n\t"
      "s_lshl_b32 s53, s52, 10\n\t"
      "s_add_u32 m0, s53, 32768\n\t"
      "s_mul_i32 s53, s52, 28672\n\t"
      "buffer_load_dwordx4 v160, s[28:31], s53 offen lds\n\t"
      "s_add_u32 s52, s52, 1\n\t"
      "s_lshl_b32 s53, s52, 10\n\t"
      "s_add_u32 m0, s53, 32768\n\t"
      "s_mul_i32 s53, s52, 28672\n\t"
      "buffer_load_dwordx4 v160, s[28:31], s53 offen lds\n\t"
      "s_add_u32 s52, s52, 1\n\t"
      "s_lshl_b32 s53, s52, 10\n\t"
      "s_add_u32 m0, s53, 32768\n\t"
      "s_mul_i32 s53, s52, 28672\n\t"
      "buffer_load_dwordx4 v160, s[28:31], s53 offen lds\n\t"
      "s_add_u32 s52, s52, 1\n\t"
      "s_lshl_b32 s53, s52, 10\n\t"
      "s_add_u32 m0, s53, 32768\n\t"
      "s_mul_i32 s53, s52, 28672\n\t"
      "buffer_load_dwordx4 v160, s[28:31], s53 offen lds\n\t"
      "s_add_u32 s52, s52, 1\n\t"
      "s_lshl_b32 s53, s52, 10\n\t"
      "s_add_u32 m0, s53, 32768\n\t"
      "s_mul_i32 s53, s52, 28672\n\t"
      "buffer_load_dwordx4 v160, s[28:31], s53 offen lds\n\t"
      "s_add_u32 s52, s52, 1\n\t"
      "s_lshl_b32 s53, s52, 10\n\t"
      "s_add_u32 m0, s53, 32768\n\t"
      "s_mul_i32 s53, s52, 28672\n\t"
      "buffer_load_dwordx4 v160, s[28:31], s53 offen lds\n\t"
      "s_add_u32 s52, s52, 1\n\t"
      "s_lshl_b32 s53, s52, 10\n\t"
      "s_add_u32 m0, s53, 32768\n\t"
      "s_mul_i32 s53, s52, 28672\n\t"
      "buffer_load_dwordx4 v160, s[28:31], s53 offen lds\n\t"
      "s_cmp_lt_u32 s40, 2\n\t"
      "s_cbranch_scc0 L_nores_%=\n\t"
      "s_lshl_b32 s52, s40, 2\n\t"
      "v_add_u32 v152, s52, v106\n\t"
      "v_lshlrev_b32 v160, 2, v152\n\t"
      "v_add_u32 v160, s50, v160\n\t"
      "ds_read_b32 v150, v160\n\t"
      "s_lshr_b32 s52, s11, 1\n\t"
      "v_add_u32 v153, s52, v105\n\t"
      "v_mul_u32_u24 v161, 9363, v153\n\t"
      "v_lshrrev_b32 v161, 20, v161\n\t"
      "v_lshlrev_b32 v162, 1, v153\n\t"
      "v_lshl_add_u32 v161, v161, 5, v162\n\t"
      "v_and_b32 v161, -4, v161\n\t"
      "s_waitcnt lgkmcnt(0)\n\t"
      "v_lshrrev_b32 v157, 16, v150\n\t"
      "v_and_b32 v150, 0x7ff, v150\n\t"
      "v_sub_u32 v150, 2047, v150\n\t"
      "v_lshl_add_u32 v161, v150, 10, v161\n\t"
      "v_mul_u32_u24 v163, 896, v150\n\t"
      "v_add_u32 v163, v163, v162\n\t"
      "v_mov_b32 v153, v163\n\t"
      "v_lshrrev_b32 v164, 3, v150\n\t"
      "v_lshlrev_b32 v164, 7, v164\n\t"
      "v_and_b32 v165, 7, v150\n\t"
      "v_lshl_add_u32 v154, v165, 2, v164\n\t"
      "s_mov_b32 s52, 5242880\n\t"
      "buffer_load_dword v140, v161, s[64:67], s52 offen sc1\n\t"
      "s_mov_b32 s52, 7340032\n\t"
      "buffer_load_dword v141, v161, s[64:67], s52 offen sc1\n\t"
      "s_mov_b32 s52, 9437184\n\t"
      "buffer_load_dword v142, v161, s[64:67], s52 offen sc1\n\t"
      "s_mov_b32 s52, 11534336\n\t"
      "buffer_load_dword v143, v161, s[64:67], s52 offen sc1\n\t"
      "buffer_load_ushort v144, v163, s[20:23], 0 offen\n\t"
      "s_mov_b32 s52, 65568\n\t"
      "buffer_load_dword v145, v154, s[64:67], s52 offen sc1\n\t"
      "s_mov_b32 s52, 131072\n\t"
      "buffer_load_dword v146, v154, s[64:67], s52 offen sc1\n\t"
      "v_and_b32 v164, 0x8000, v157\n\t"
      "v_cmp_ne_u32 vcc, 0, v164\n\t"
      "v_and_b32 v164, 0x7fff, v157\n\t"
      "v_xor_b32 v165, 0xffff, v157\n\t"
      "v_cndmask_b32 v157, v165, v164, vcc\n\t"
      "v_lshlrev_b32 v157, 16, v157\n\t"
      "v_mul_f32 v157, 0xbfb8aa3b, v157\n\t"
      "v_exp_f32 v157, v157\n\t"
      "s_nop 0\n\t"
      "v_add_f32 v157, 1.0, v157\n\t"
      "v_rcp_f32 v157, v157\n\t"
      "s_nop 0\n\t"
      "v_cvt_pk_bf16_f32 v157, v157, v157\n\t"
      "v_lshlrev_b32 v157, 16, v157\n\t"
      "v_and_b32 v164, 1, v105\n\t"
      "v_cmp_eq_u32 s[68:69], 0, v164\n\t"
      "s_waitcnt vmcnt(0)\n\t"
      "v_lshlrev_b32 v164, 16, v140\n\t"
      "v_and_b32 v140, 0xffff0000, v140\n\t"
      "v_cndmask_b32 v140, v140, v164, s[68:69]\n\t"
      "v_lshlrev_b32 v164, 16, v141\n\t"
      "v_and_b32 v141, 0xffff0000, v141\n\t"
      "v_cndmask_b32 v141, v141, v164, s[68:69]\n\t"
      "v_lshlrev_b32 v164, 16, v142\n\t"
      "v_and_b32 v142, 0xffff0000, v142\n\t"
      "v_cndmask_b32 v142, v142, v164, s[68:69]\n\t"
      "v_lshlrev_b32 v164, 16, v143\n\t"
      "v_and_b32 v143, 0xffff0000, v143\n\t"
      "v_cndmask_b32 v143, v143, v164, s[68:69]\n\t"
      "v_add_f32 v140, v140, v141\n\t"
      "v_add_f32 v142, v142, v143\n\t"
      "v_sub_f32 v140, v140, v142\n\t"
      "v_cvt_pk_bf16_f32 v140, v140, v140\n\t"
      "v_lshlrev_b32 v140, 16, v140\n\t"
      "v_lshlrev_b32 v144, 16, v144\n\t"
      "v_add_f32 v155, v140, v144\n\t"
      "v_cvt_pk_bf16_f32 v155, v155, v155\n\t"
      "v_lshlrev_b32 v155, 16, v155\n\t"
      "v_lshlrev_b32 v164, 16, v145\n\t"
      "v_and_b32 v165, 0xffff0000, v145\n\t"
      "v_rcp_f32 v165, v165\n\t"
      "v_sub_f32 v156, v155, v164\n\t"
      "v_cvt_pk_bf16_f32 v156, v156, v156\n\t"
      "v_lshlrev_b32 v156, 16, v156\n\t"
      "v_mul_f32 v156, v156, v165\n\t"
      "v_cvt_pk_bf16_f32 v156, v156, v156\n\t"
      "v_lshlrev_b32 v156, 16, v156\n\t"
      "v_mov_b32 v167, v146\n\t"
      "v_mov_b32 v158, v167\n\t"
      "L_nores_%=:\n\t"
      "s_waitcnt vmcnt(0)\n\t"
      "s_barrier\n\t"
      "s_mul_i32 s52, s40, 7168\n\t"
      "s_add_u32 s52, s52, 32768\n\t"
      "v_add_u32 v160, s52, v245\n\t"
      "ds_read_b64_tr_b16 v[176:177], v160 offset:0\n\t"
      "ds_read_b64_tr_b16 v[178:179], v160 offset:128\n\t"
      "ds_read_b64_tr_b16 v[180:181], v160 offset:1024\n\t"
      "ds_read_b64_tr_b16 v[182:183], v160 offset:1152\n\t"
      "ds_read_b64_tr_b16 v[184:185], v160 offset:2048\n\t"
      "ds_read_b64_tr_b16 v[186:187], v160 offset:2176\n\t"
      "ds_read_b64_tr_b16 v[188:189], v160 offset:3072\n\t"
      "ds_read_b64_tr_b16 v[190:191], v160 offset:3200\n\t"
      "ds_read_b64_tr_b16 v[192:193], v160 offset:4096\n\t"
      "ds_read_b64_tr_b16 v[194:195], v160 offset:4224\n\t"
      "ds_read_b64_tr_b16 v[196:197], v160 offset:5120\n\t"
      "ds_read_b64_tr_b16 v[198:199], v160 offset:5248\n\t"
      "ds_read_b64_tr_b16 v[200:201], v160 offset:6144\n\t"
      "ds_read_b64_tr_b16 v[202:203], v160 offset:6272\n\t"
      "v_lshrrev_b32 v161, 5, v104\n\t"
      "v_lshlrev_b32 v161, 8, v161\n\t"
      "v_and_b32 v162, 7, v104\n\t"
      "v_lshl_add_u32 v161, v162, 5, v161\n\t"
      "v_and_b32 v162, 1, v106\n\t"
      "v_lshl_add_u32 v159, v162, 4, v161\n\t"
      "s_mul_i32 s54, s40, 3584\n\t"
      "s_add_u32 s54, s54, 98304\n\t"
      "buffer_load_dwordx4 v[208:211], v159, s[64:67], s54 offen offset:0 sc1\n\t"
      "buffer_load_dwordx4 v[212:215], v159, s[64:67], s54 offen offset:512 sc1\n\t"
      "buffer_load_dwordx4 v[216:219], v159, s[64:67], s54 offen offset:1024 sc1\n\t"
      "buffer_load_dwordx4 v[220:223], v159, s[64:67], s54 offen offset:1536 sc1\n\t"
      "buffer_load_dwordx4 v[224:227], v159, s[64:67], s54 offen offset:2048 sc1\n\t"
      "buffer_load_dwordx4 v[228:231], v159, s[64:67], s54 offen offset:2560 sc1\n\t"
      "buffer_load_dwordx4 v[232:235], v159, s[64:67], s54 offen offset:3072 sc1\n\t"
      "s_mov_b32 s14, 0\n\t"
      "L_ap_%=:\n\t"
      "s_waitcnt vmcnt(0)\n\t"
      "v_max3_u32 v161, v208, v209, v210\n\t"
      "v_max3_u32 v161, v211, v212, v161\n\t"
      "v_max3_u32 v161, v213, v214, v161\n\t"
      "v_max3_u32 v161, v215, v216, v161\n\t"
      "v_max3_u32 v161, v217, v218, v161\n\t"
      "v_max3_u32 v161, v219, v220, v161\n\t"
      "v_max3_u32 v161, v221, v222, v161\n\t"
      "v_max3_u32 v161, v223, v224, v161\n\t"
      "v_max3_u32 v161, v225, v226, v161\n\t"
      "v_max3_u32 v161, v227, v228, v161\n\t"
      "v_max3_u32 v161, v229, v230, v161\n\t"
      "v_max3_u32 v161, v231, v232, v161\n\t"
      "v_max3_u32 v161, v233, v234, v161\n\t"
      "v_max_u32 v161, v235, v161\n\t"
      "v_cmp_eq_u32 vcc, -1, v161\n\t"
      "s_cbranch_vccz L_apx_%=\n\t"
      "s_add_u32 s14, s14, 1\n\t"
      "s_cmp_lt_u32 s14, 65536\n\t"
      "s_cbranch_scc0 L_apx_%=\n\t"
      "s_mov_b64 exec, vcc\n\t"
      "buffer_load_dwordx4 v[208:211], v159, s[64:67], s54 offen offset:0 sc1\n\t"
      "buffer_load_dwordx4 v[212:215], v159, s[64:67], s54 offen offset:512 sc1\n\t"
      "buffer_load_dwordx4 v[216:219], v159, s[64:67], s54 offen offset:1024 sc1\n\t"
      "buffer_load_dwordx4 v[220:223], v159, s[64:67], s54 offen offset:1536 sc1\n\t"
      "buffer_load_dwordx4 v[224:227], v159, s[64:67], s54 offen offset:2048 sc1\n\t"
      "buffer_load_dwordx4 v[228:231], v159, s[64:67], s54 offen offset:2560 sc1\n\t"
      "buffer_load_dwordx4 v[232:235], v159, s[64:67], s54 offen offset:3072 sc1\n\t"
      "s_mov_b64 exec, -1\n\t"
      "s_branch L_ap_%=\n\t"
      "L_apx_%=:\n\t"
      "s_waitcnt lgkmcnt(0)\n\t"
      "v_mfma_f32_16x16x32_bf16 v[240:243], v[208:211], v[176:179], 0\n\t"
      "v_mfma_f32_16x16x32_bf16 v[240:243], v[212:215], v[180:183], v[240:243]\n\t"
      "v_mfma_f32_16x16x32_bf16 v[240:243], v[216:219], v[184:187], v[240:243]\n\t"
      "v_mfma_f32_16x16x32_bf16 v[240:243], v[220:223], v[188:191], v[240:243]\n\t"
      "v_mfma_f32_16x16x32_bf16 v[240:243], v[224:227], v[192:195], v[240:243]\n\t"
      "v_mfma_f32_16x16x32_bf16 v[240:243], v[228:231], v[196:199], v[240:243]\n\t"
      "v_mfma_f32_16x16x32_bf16 v[240:243], v[232:235], v[200:203], v[240:243]\n\t"
      "s_lshl_b32 s52, s40, 10\n\t"
      "s_add_u32 s52, s52, 8704\n\t"
      "v_add_u32 v160, s52, v107\n\t"
      "s_nop 5\n\t"
      "ds_write_b128 v160, v[240:243]\n\t"
      "s_cmp_lt_u32 s40, 2\n\t"
      "s_cbranch_scc0 L_odx_%=\n\t"
      "s_mov_b32 s14, 0\n\t"
      "L_od_%=:\n\t"
      "v_cmp_eq_u32 vcc, -1, v158\n\t"
      "s_cbranch_vccz L_odx_%=\n\t"
      "s_add_u32 s14, s14, 1\n\t"
      "s_cmp_lt_u32 s14, 0x10000\n\t"
      "s_cbranch_scc0 L_odx_%=\n\t"
      "s_mov_b32 s52, 131072\n\t"
      "buffer_load_dword v158, v154, s[64:67], s52 offen sc1\n\t"
      "s_waitcnt vmcnt(0)\n\t"
      "s_branch L_od_%=\n\t"
      "L_odx_%=:\n\t"
      "s_waitcnt lgkmcnt(0)\n\t"
      "s_barrier\n\t"
      "s_cmp_lt_u32 s40, 2\n\t"
      "s_cbranch_scc0 L_end19b_%=\n\t"
      "v_lshrrev_b32 v160, 2, v152\n\t"
      "v_lshl_add_u32 v160, v160, 4, v105\n\t"
      "v_lshlrev_b32 v160, 4, v160\n\t"
      "v_and_b32 v161, 3, v152\n\t"
      "v_lshl_add_u32 v160, v161, 2, v160\n\t"
      "ds_read_b32 v140, v160 offset:8704\n\t"
      "ds_read_b32 v141, v160 offset:9728\n\t"
      "ds_read_b32 v142, v160 offset:10752\n\t"
      "ds_read_b32 v143, v160 offset:11776\n\t"
      "ds_read_b32 v144, v160 offset:12800\n\t"
      "ds_read_b32 v145, v160 offset:13824\n\t"
      "ds_read_b32 v146, v160 offset:14848\n\t"
      "ds_read_b32 v147, v160 offset:15872\n\t"
      "s_waitcnt lgkmcnt(0)\n\t"
      "v_add_f32 v140, v140, v141\n\t"
      "v_add_f32 v142, v142, v143\n\t"
      "v_add_f32 v144, v144, v145\n\t"
      "v_add_f32 v146, v146, v147\n\t"
      "v_add_f32 v140, v140, v142\n\t"
      "v_add_f32 v144, v144, v146\n\t"
      "v_add_f32 v140, v140, v144\n\t"
      "v_cvt_pk_bf16_f32 v140, v140, v140\n\t"
      "v_lshlrev_b32 v140, 16, v140\n\t"
      "v_mul_f32 v140, v140, v157\n\t"
      "v_cvt_pk_bf16_f32 v140, v140, v140\n\t"
      "v_lshlrev_b32 v140, 16, v140\n\t"
      "v_add_f32 v140, v156, v140\n\t"
      "v_cvt_pk_bf16_f32 v140, v140, v140\n\t"
      "v_lshlrev_b32 v140, 16, v140\n\t"
      "v_add_f32 v140, v155, v140\n\t"
      "v_cvt_pk_bf16_f32 v140, v140, v140\n\t"
      "buffer_store_short v140, v153, s[24:27], 0 offen\n\t"
      "L_end19b_%=:\n\t"
      "L_end_%=:\n\t"
      : "+{v0}"(tid)
      : [ka] "s"(ka), [bid] "s"(blockIdx.x)
      : "v1", "v2", "v3", "v4", "v5", "v6", "v7", "v8", "v9", "v10", "v11", "v12", "v13", "v14", "v15", "v16", "v17", "v18", "v19", "v20", "v21", "v22", "v23", "v24", "v25", "v26", "v27", "v28", "v29", "v30", "v31", "v32", "v33", "v34", "v35", "v36", "v37", "v38", "v39", "v40", "v41", "v42", "v43", "v44", "v45", "v46", "v47", "v48", "v49", "v50", "v51", "v52", "v53", "v54", "v55", "v56", "v57", "v58", "v59", "v60", "v61", "v62", "v63", "v64", "v65", "v66", "v67", "v68", "v69", "v70", "v71", "v72", "v73", "v74", "v75", "v76", "v77", "v78", "v79", "v80", "v81", "v82", "v83", "v84", "v85", "v86", "v87", "v88", "v89", "v90", "v91", "v92", "v93", "v94", "v95", "v96", "v97", "v98", "v99", "v100", "v101", "v102", "v103", "v104", "v105", "v106", "v107", "v108", "v109", "v110", "v111", "v112", "v113", "v114", "v115", "v116", "v117", "v118", "v119", "v120", "v121", "v122", "v123", "v124", "v125", "v126", "v127", "v128", "v129", "v130", "v131", "v132", "v133", "v134", "v135", "v136", "v137", "v138", "v139", "v140", "v141", "v142", "v143", "v144", "v145", "v146", "v147", "v148", "v149", "v150", "v151", "v152", "v153", "v154", "v155", "v156", "v157", "v158", "v159", "v160", "v161", "v162", "v163", "v164", "v165", "v166", "v167", "v168", "v169", "v170", "v171", "v172", "v173", "v174", "v175", "v176", "v177", "v178", "v179", "v180", "v181", "v182", "v183", "v184", "v185", "v186", "v187", "v188", "v189", "v190", "v191", "v192", "v193", "v194", "v195", "v196", "v197", "v198", "v199", "v200", "v201", "v202", "v203", "v204", "v205", "v206", "v207", "v208", "v209", "v210", "v211", "v212", "v213", "v214", "v215", "v216", "v217", "v218", "v219", "v220", "v221", "v222", "v223", "v224", "v225", "v226", "v227", "v228", "v229", "v230", "v231", "v232", "v233", "v234", "v235", "v236", "v237", "v238", "v239", "v240", "v241", "v242", "v243", "v244", "v245", "v246", "v247", "v248", "v249", "v250", "v251", "v252", "v253", "v254", "v255", "s10", "s11", "s12", "s13", "s14", "s15", "s16", "s17", "s18", "s19", "s20", "s21", "s22", "s23", "s24", "s25", "s26", "s27", "s28", "s29", "s30", "s31", "s34", "s35", "s36", "s37", "s38", "s39", "s40", "s41", "s42", "s43", "s44", "s45", "s46", "s47", "s48", "s49", "s50", "s51", "s52", "s53", "s54", "s55", "s56", "s57", "s58", "s59", "s60", "s61", "s62", "s63", "s64", "s65", "s66", "s67", "s68", "s69", "s70", "s71", "s72", "s73", "s74", "s75", "s76", "s77", "s78", "s79", "s80", "s81", "s82", "s83", "s84", "s85", "s86", "s87", "s88", "s89", "s90", "s91", "s92", "s93", "s94", "s95", "s96", "s97", "s98", "s99", "vcc", "scc", "memory");
  asm volatile("L_kend:");
}

extern "C" void kernel_launch(void* const* d_in, const int* in_sizes, int n_in, void* d_out, int out_size, void* d_ws, size_t ws_size,
                              hipStream_t stream) {
  Args a;
  a.x = (const u16*)d_in[0];
  a.wsre = (const u16*)d_in[1];
  a.wsim = (const u16*)d_in[2];
  a.wre = (const u16*)d_in[3];
  a.wim = (const u16*)d_in[4];
  a.ws = (unsigned char*)d_ws;
  a.wr = (const u16*)d_in[6];
  a.out = (u16*)d_out;
  a.wg = (const u16*)d_in[7];
  a.wu = (const u16*)d_in[8];
  a.wd = (const u16*)d_in[9];
  (void)hipFuncSetAttribute((const void*)fnet_moe_kernel, hipFuncAttributeMaxDynamicSharedMemorySize, 162048);
  fnet_moe_kernel<<<256, 512, 162048, stream>>>(a);
}
```
